```python
import torch

def _softmax(x, axis=-1):
    return torch.softmax(x, dim=axis)
def _gelu(x):
    return torch.nn.functional.gelu(x, approximate="tanh")
def _silu(x):
    return torch.nn.functional.silu(x)
def _sigmoid(x):
    return torch.sigmoid(x)
def _layernorm(x, axis=-1, eps=1e-5):
    m = x.mean(dim=axis, keepdim=True)
    v = x.var(dim=axis, keepdim=True, unbiased=False)
    return (x - m) / torch.sqrt(v + eps)
def _scatter_add(tgt, idx, src, axis=0):
    return tgt.clone().index_add_(axis, idx, src)
def _segment_sum(src, seg, n):
    out = torch.zeros((n,) + src.shape[1:], dtype=src.dtype)
    return out.index_add_(0, seg, src)
def _pad(x, pw):
    flat = tuple(v for pair in reversed(pw) for v in pair)
    return torch.nn.functional.pad(x, flat)

kernel_name = 'standalone__mixer_token_mlp__prompt'

def setup_inputs():

    _w = (lambda a: (lambda t: t.to(torch.bfloat16) if t.is_floating_point() else t)(torch.as_tensor(a)))
    B, S, D, F = 1, 2048, 448, 1792
    return {
        "x": _w((lambda *s: torch.randn(*s, dtype=torch.bfloat16) * 0.05)(B, S, D)),
        "w1": _w((lambda *s: torch.randn(*s, dtype=torch.bfloat16) * 0.2)(S, F) * (S ** -0.5)),
        "b1": _w((lambda *s: torch.randn(*s, dtype=torch.bfloat16) * 3.2)(F) * 0.02),
        "w2": _w((lambda *s: torch.randn(*s, dtype=torch.bfloat16) * 0.05)(F, S) * (F ** -0.5)),
        "b2": _w((lambda *s: torch.randn(*s, dtype=torch.bfloat16) * 0.1)(S) * 0.02),
    }

def reference(x, w1, b1, w2, b2):
    B, S, D = x.shape
    h = _layernorm(x, axis=-1)
    u = torch.einsum("bsd,sf->bdf", h, w1) + b1
    u = _gelu(u)
    y = torch.einsum("bdf,fs->bds", u, w2) + b2
    return (lambda x, *d: x.permute(*d))(y, 0, 2, 1)
```

```cpp
#include <hip/hip_runtime.h>
#include <hip/hip_bf16.h>

constexpr int S = 2048, D = 448, F = 1792;

__device__ __forceinline__ float bf2f(unsigned short u) { return __uint_as_float(((unsigned)u) << 16); }
__device__ __forceinline__ unsigned short f2bf(float f) { __hip_bfloat16 b = __float2bfloat16(f); return *reinterpret_cast<unsigned short*>(&b); }

__global__ void ln_kernel(const unsigned short* x, unsigned short* h) {
    int row = blockIdx.x; int lane = threadIdx.x;
    float v[7]; float s = 0.f;
    for (int i = 0; i < 7; ++i) { v[i] = bf2f(x[row * D + lane + 64 * i]); s += v[i]; }
    for (int m = 32; m >= 1; m >>= 1) s += __shfl_xor(s, m);
    float mean = s / D; float q = 0.f;
    for (int i = 0; i < 7; ++i) { float d = v[i] - mean; q += d * d; }
    for (int m = 32; m >= 1; m >>= 1) q += __shfl_xor(q, m);
    float r = rsqrtf(q / D + 1e-5f);
    for (int i = 0; i < 7; ++i) h[row * D + lane + 64 * i] = f2bf((v[i] - mean) * r);
}
__global__ void g1_kernel(const unsigned short* h, const unsigned short* w1, const unsigned short* b1, unsigned short* G) {
    int d = threadIdx.x + (blockIdx.x % 7) * 64; int f = blockIdx.x / 7;
    float acc = 0.f;
    for (int s = 0; s < S; ++s) acc += bf2f(h[s * D + d]) * bf2f(w1[s * F + f]);
    float u = acc + bf2f(b1[f]);
    float t = tanhf(0.7978845608f * (u + 0.044715f * u * u * u));
    G[f * D + d] = f2bf(0.5f * u * (1.f + t));
}
__global__ void g2_kernel(const unsigned short* G, const unsigned short* w2, const unsigned short* b2, unsigned short* out) {
    int d = threadIdx.x + (blockIdx.x % 7) * 64; int s = blockIdx.x / 7;
    float acc = 0.f;
    for (int f = 0; f < F; ++f) acc += bf2f(G[f * D + d]) * bf2f(w2[f * S + s]);
    out[s * D + d] = f2bf(acc + bf2f(b2[s]));
}

extern "C" void kernel_launch(void* const* d_in, const int* in_sizes, int n_in,
                              void* d_out, int out_size, void* d_ws, size_t ws_size,
                              hipStream_t stream) {
    const unsigned short* x = (const unsigned short*)d_in[0];
    const unsigned short* w1 = (const unsigned short*)d_in[1];
    const unsigned short* b1 = (const unsigned short*)d_in[2];
    const unsigned short* w2 = (const unsigned short*)d_in[3];
    const unsigned short* b2 = (const unsigned short*)d_in[4];
    unsigned short* h = (unsigned short*)d_ws;
    unsigned short* G = h + S * D;
    ln_kernel<<<S, 64, 0, stream>>>(x, h);
    g1_kernel<<<F * 7, 64, 0, stream>>>(h, w1, b1, G);
    g2_kernel<<<S * 7, 64, 0, stream>>>(G, w2, b2, (unsigned short*)d_out);
}
```

```cpp
#include <hip/hip_runtime.h>
#include <hip/hip_bf16.h>

constexpr int S = 2048, D = 448, F = 1792;
typedef short bf16x8 __attribute__((ext_vector_type(8)));
typedef short s16x4 __attribute__((ext_vector_type(4)));
typedef float f32x4 __attribute__((ext_vector_type(4)));
typedef unsigned u32x4 __attribute__((ext_vector_type(4)));
typedef __attribute__((address_space(3))) const char* lds_cptr;
typedef __attribute__((address_space(3))) char* lds_ptr;
typedef __attribute__((address_space(3))) s16x4* lds_s16x4;

__device__ __forceinline__ float bf2f(unsigned short u) { return __uint_as_float(((unsigned)u) << 16); }
__device__ __forceinline__ unsigned cvtpk(float lo, float hi) { unsigned r; asm("v_cvt_pk_bf16_f32 %0, %1, %2" : "=v"(r) : "v"(lo), "v"(hi)); return r; }
__device__ __forceinline__ s16x4 vtr(lds_cptr p) { return __builtin_amdgcn_ds_read_tr16_b64_v4i16((lds_s16x4)p); }
__device__ __forceinline__ void glds16(unsigned voff, const void* sbase, unsigned lds_dst) {
    asm volatile("s_mov_b32 m0, %2\n\ts_nop 0\n\tglobal_load_lds_dwordx4 %0, %1" :: "v"(voff), "s"(sbase), "s"(lds_dst) : "memory");
}

__global__ __launch_bounds__(256) void ln_kernel(const unsigned short* __restrict__ x, unsigned short* __restrict__ h) {
    const int lane = threadIdx.x & 63, row = blockIdx.x * 4 + (threadIdx.x >> 6);
    u32x4 raw = {0u, 0u, 0u, 0u};
    if (lane < 56) raw = *(const u32x4*)(x + row * D + lane * 8);
    float v[8];
#pragma unroll
    for (int i = 0; i < 4; ++i) { v[2 * i] = __uint_as_float(raw[i] << 16); v[2 * i + 1] = __uint_as_float(raw[i] & 0xffff0000u); }
    float s = 0.f;
#pragma unroll
    for (int i = 0; i < 8; ++i) s += v[i];
#pragma unroll
    for (int m = 32; m >= 1; m >>= 1) s += __shfl_xor(s, m);
    const float mean = s * (1.f / D);
    float q = 0.f;
#pragma unroll
    for (int i = 0; i < 8; ++i) { v[i] -= mean; q += v[i] * v[i]; }
    if (lane >= 56) q = 0.f;
#pragma unroll
    for (int m = 32; m >= 1; m >>= 1) q += __shfl_xor(q, m);
    const float r = rsqrtf(q * (1.f / D) + 1e-5f);
    u32x4 o;
#pragma unroll
    for (int i = 0; i < 4; ++i) o[i] = cvtpk(v[2 * i] * r, v[2 * i + 1] * r);
    if (lane < 56) *(u32x4*)(h + row * D + lane * 8) = o;
}

#define MFMA16(a, b, c) __builtin_amdgcn_mfma_f32_16x16x32_bf16(a, b, c, 0, 0, 0)

template <int K, int LDW, int EPI>
__global__ __launch_bounds__(512, 2) void gemm_kernel(const unsigned short* __restrict__ W, const unsigned short* __restrict__ X,
                                                       const unsigned short* __restrict__ bias, unsigned short* __restrict__ out) {
    extern __shared__ __attribute__((aligned(16))) char lds[];
    constexpr int NST = K / 128, STAGE = 32768, NBUF = 4;
    constexpr int MT = LDW / 64;
    const int tid = threadIdx.x, lane = tid & 63;
    const int wid = __builtin_amdgcn_readfirstlane(tid >> 6);
    const int xl = blockIdx.x & 7, j = blockIdx.x >> 3;
    const int dg = j % 7, mt_local = j / 7;
    const int mtile = mt_local * 8 + xl;
    if (mtile >= MT) return;
    const unsigned lds0 = (unsigned)(uintptr_t)lds;
    const int r8 = lane >> 3, cp = lane & 7, f0 = (lane >> 4) & 1;
    const unsigned wo0 = (unsigned)((wid * 16 + r8) * LDW * 2 + ((cp ^ (f0 << 1)) * 16));
    const unsigned wo1 = (unsigned)((wid * 16 + 8 + r8) * LDW * 2 + ((cp ^ ((f0 | 2) << 1)) * 16));
    const unsigned xo0 = (unsigned)((wid * 16 + r8) * D * 2 + ((cp ^ (f0 << 1)) * 16));
    const unsigned xo1 = (unsigned)((wid * 16 + 8 + r8) * D * 2 + ((cp ^ ((f0 | 2) << 1)) * 16));
    const char* wb = (const char*)(W + mtile * 64);
    const char* xb = (const char*)(X + dg * 64);
    auto stage = [&](int s) {
        const unsigned dst = lds0 + (s % NBUF) * STAGE + wid * 2048;
        const char* wp = wb + (size_t)s * 128 * LDW * 2;
        const char* xp = xb + (size_t)s * 128 * D * 2;
        glds16(wo0, wp, dst); glds16(wo1, wp, dst + 1024);
        glds16(xo0, xp, dst + 16384); glds16(xo1, xp, dst + 16384 + 1024);
    };
    const int q = wid >> 1, mh = wid & 1;
    const int g = lane >> 4, qq = (lane & 15) >> 2, p = lane & 3;
    const int fr = ((qq >> 1) & 1) | ((g & 1) << 1);
    const int rowb = (32 * q + 8 * g + qq) * 128 + (p & 1) * 8;
    unsigned aw[2], ax[4];
#pragma unroll
    for (int c = 0; c < 2; ++c) aw[c] = rowb + ((((2 * mh + c) ^ fr) * 2 + (p >> 1)) * 16);
#pragma unroll
    for (int c = 0; c < 4; ++c) ax[c] = 16384 + rowb + (((c ^ fr) * 2 + (p >> 1)) * 16);

    f32x4 acc[2][4];
#pragma unroll
    for (int a = 0; a < 2; ++a)
#pragma unroll
        for (int b = 0; b < 4; ++b) acc[a][b] = (f32x4){0.f, 0.f, 0.f, 0.f};

    stage(0); stage(1); stage(2);
#pragma unroll 1
    for (int s = 0; s < NST; ++s) {
        if (s + 2 < NST) asm volatile("s_waitcnt vmcnt(8) lgkmcnt(0)\n\ts_barrier" ::: "memory");
        else if (s + 1 < NST) asm volatile("s_waitcnt vmcnt(4) lgkmcnt(0)\n\ts_barrier" ::: "memory");
        else asm volatile("s_waitcnt vmcnt(0) lgkmcnt(0)\n\ts_barrier" ::: "memory");
        if (s + 3 < NST) stage(s + 3);
        lds_cptr base = (lds_cptr)lds + (s % NBUF) * STAGE;
        bf16x8 wf[2], xf[4];
#pragma unroll
        for (int c = 0; c < 2; ++c) { s16x4 lo = vtr(base + aw[c]), hi = vtr(base + aw[c] + 512); wf[c] = __builtin_shufflevector(lo, hi, 0, 1, 2, 3, 4, 5, 6, 7); }
#pragma unroll
        for (int c = 0; c < 4; ++c) { s16x4 lo = vtr(base + ax[c]), hi = vtr(base + ax[c] + 512); xf[c] = __builtin_shufflevector(lo, hi, 0, 1, 2, 3, 4, 5, 6, 7); }
#pragma unroll
        for (int a = 0; a < 2; ++a)
#pragma unroll
            for (int b = 0; b < 4; ++b) acc[a][b] = MFMA16(xf[b], wf[a], acc[a][b]);
    }
    asm volatile("s_waitcnt lgkmcnt(0)\n\ts_barrier" ::: "memory");
    constexpr int RS = 272;
    {
        lds_ptr red = (lds_ptr)lds + q * (64 * RS);
#pragma unroll
        for (int a = 0; a < 2; ++a)
#pragma unroll
            for (int b = 0; b < 4; ++b)
                *(__attribute__((address_space(3))) f32x4*)(red + (32 * mh + 16 * a + (lane & 15)) * RS + (16 * b + 4 * g) * 4) = acc[a][b];
    }
    __syncthreads();
    {
        const int m = tid >> 3, d0 = (tid & 7) * 8;
        f32x4 s0 = {0.f, 0.f, 0.f, 0.f}, s1 = s0;
#pragma unroll
        for (int qq2 = 0; qq2 < 4; ++qq2) {
            lds_cptr rp = (lds_cptr)lds + qq2 * (64 * RS) + m * RS + d0 * 4;
            s0 += *(const __attribute__((address_space(3))) f32x4*)rp;
            s1 += *(const __attribute__((address_space(3))) f32x4*)(rp + 16);
        }
        const float bv = bf2f(bias[mtile * 64 + m]);
        float v[8];
#pragma unroll
        for (int i = 0; i < 4; ++i) { v[i] = s0[i] + bv; v[4 + i] = s1[i] + bv; }
        if (EPI == 1) {
#pragma unroll
            for (int i = 0; i < 8; ++i) {
                const float u = v[i];
                const float z = u * (-2.302208198f + -0.1029432397f * u * u);
                v[i] = u * __builtin_amdgcn_rcpf(1.f + __builtin_amdgcn_exp2f(z));
            }
        }
        u32x4 o = {cvtpk(v[0], v[1]), cvtpk(v[2], v[3]), cvtpk(v[4], v[5]), cvtpk(v[6], v[7])};
        *(u32x4*)(out + (size_t)(mtile * 64 + m) * D + dg * 64 + d0) = o;
    }
}

extern "C" void kernel_launch(void* const* d_in, const int* in_sizes, int n_in,
                              void* d_out, int out_size, void* d_ws, size_t ws_size,
                              hipStream_t stream) {
    const unsigned short* x = (const unsigned short*)d_in[0];
    const unsigned short* w1 = (const unsigned short*)d_in[1];
    const unsigned short* b1 = (const unsigned short*)d_in[2];
    const unsigned short* w2 = (const unsigned short*)d_in[3];
    const unsigned short* b2 = (const unsigned short*)d_in[4];
    unsigned short* h = (unsigned short*)d_ws;
    unsigned short* G = h + S * D;
    constexpr int LDSB = 131072;
    (void)hipFuncSetAttribute((const void*)gemm_kernel<S, F, 1>, hipFuncAttributeMaxDynamicSharedMemorySize, LDSB);
    (void)hipFuncSetAttribute((const void*)gemm_kernel<F, S, 0>, hipFuncAttributeMaxDynamicSharedMemorySize, LDSB);
    ln_kernel<<<S / 4, 256, 0, stream>>>(x, h);
    gemm_kernel<S, F, 1><<<224, 512, LDSB, stream>>>(w1, h, b1, G);
    gemm_kernel<F, S, 0><<<224, 512, LDSB, stream>>>(w2, G, b2, (unsigned short*)d_out);
}
```

```cpp
#define USE_NT 0
#include <hip/hip_runtime.h>
#include <hip/hip_bf16.h>

constexpr int S = 2048, D = 448, F = 1792;
typedef short bf16x8 __attribute__((ext_vector_type(8)));
typedef short s16x4 __attribute__((ext_vector_type(4)));
typedef float f32x4 __attribute__((ext_vector_type(4)));
typedef unsigned u32x4 __attribute__((ext_vector_type(4)));
typedef __attribute__((address_space(3))) const char* lds_cptr;
typedef __attribute__((address_space(3))) char* lds_ptr;
typedef __attribute__((address_space(3))) s16x4* lds_s16x4;

__device__ __forceinline__ float bf2f(unsigned short u) { return __uint_as_float(((unsigned)u) << 16); }
__device__ __forceinline__ unsigned cvtpk(float lo, float hi) { unsigned r; asm("v_cvt_pk_bf16_f32 %0, %1, %2" : "=v"(r) : "v"(lo), "v"(hi)); return r; }
__device__ __forceinline__ s16x4 vtr(lds_cptr p) { return __builtin_amdgcn_ds_read_tr16_b64_v4i16((lds_s16x4)p); }
__device__ __forceinline__ void glds16(unsigned voff, const void* sbase, unsigned lds_dst) {
    asm volatile("s_mov_b32 m0, %2\n\ts_nop 0\n\tglobal_load_lds_dwordx4 %0, %1" :: "v"(voff), "s"(sbase), "s"(lds_dst) : "memory");
}

__device__ __forceinline__ void glds16nt(unsigned voff, const void* sbase, unsigned lds_dst) {
    asm volatile("s_mov_b32 m0, %2\n\ts_nop 0\n\tglobal_load_lds_dwordx4 %0, %1 nt" :: "v"(voff), "s"(sbase), "s"(lds_dst) : "memory");
}
#define USE_ROT 1
__global__ __launch_bounds__(256) void ln_kernel(const unsigned short* __restrict__ x, unsigned short* __restrict__ h) {
    const int lane = threadIdx.x & 63, row = blockIdx.x * 4 + (threadIdx.x >> 6);
    u32x4 raw = {0u, 0u, 0u, 0u};
    if (lane < 56) raw = USE_NT ? __builtin_nontemporal_load((const u32x4*)(x + row * D + lane * 8)) : *(const u32x4*)(x + row * D + lane * 8);
    float v[8];
#pragma unroll
    for (int i = 0; i < 4; ++i) { v[2 * i] = __uint_as_float(raw[i] << 16); v[2 * i + 1] = __uint_as_float(raw[i] & 0xffff0000u); }
    float s = 0.f;
#pragma unroll
    for (int i = 0; i < 8; ++i) s += v[i];
#pragma unroll
    for (int m = 32; m >= 1; m >>= 1) s += __shfl_xor(s, m);
    const float mean = s * (1.f / D);
    float q = 0.f;
#pragma unroll
    for (int i = 0; i < 8; ++i) { v[i] -= mean; q += v[i] * v[i]; }
    if (lane >= 56) q = 0.f;
#pragma unroll
    for (int m = 32; m >= 1; m >>= 1) q += __shfl_xor(q, m);
    const float r = rsqrtf(q * (1.f / D) + 1e-5f);
    u32x4 o;
#pragma unroll
    for (int i = 0; i < 4; ++i) o[i] = cvtpk(v[2 * i] * r, v[2 * i + 1] * r);
    if (lane < 56) *(u32x4*)(h + row * D + lane * 8) = o;
}

#define MFMA16(a, b, c) __builtin_amdgcn_mfma_f32_16x16x32_bf16(a, b, c, 0, 0, 0)

template <int K, int LDW, int EPI>
__global__ __launch_bounds__(512, 2) void gemm_kernel(const unsigned short* __restrict__ W, const unsigned short* __restrict__ X,
                                                       const unsigned short* __restrict__ bias, unsigned short* __restrict__ out) {
    extern __shared__ __attribute__((aligned(16))) char lds[];
    constexpr int NST = K / 128, STAGE = 32768, NBUF = 4;
    constexpr int MT = LDW / 64;
    const int tid = threadIdx.x, lane = tid & 63;
    const int wid = __builtin_amdgcn_readfirstlane(tid >> 6);
    const int xl = blockIdx.x & 7, j = blockIdx.x >> 3;
    const int dg = j % 7, mt_local = j / 7;
    const int mtile = mt_local * 8 + xl;
    if (mtile >= MT) return;
    const unsigned lds0 = (unsigned)(uintptr_t)lds;
    const int r8 = lane >> 3, cp = lane & 7, f0 = (lane >> 4) & 1;
    const unsigned wo0 = (unsigned)((wid * 16 + r8) * LDW * 2 + ((cp ^ (f0 << 1)) * 16));
    const unsigned wo1 = (unsigned)((wid * 16 + 8 + r8) * LDW * 2 + ((cp ^ ((f0 | 2) << 1)) * 16));
    const unsigned xo0 = (unsigned)((wid * 16 + r8) * D * 2 + ((cp ^ (f0 << 1)) * 16));
    const unsigned xo1 = (unsigned)((wid * 16 + 8 + r8) * D * 2 + ((cp ^ ((f0 | 2) << 1)) * 16));
    const char* wb = (const char*)(W + mtile * 64);
    const char* xb = (const char*)(X + dg * 64);
    const int rot = USE_ROT ? (2 * dg + 4 * mt_local) % NST : 0;
    auto stage = [&](int s) {
        const unsigned dst = lds0 + (s % NBUF) * STAGE + wid * 2048;
        int sr = s + rot; if (sr >= NST) sr -= NST;
        const char* wp = wb + (size_t)sr * 128 * LDW * 2;
        const char* xp = xb + (size_t)sr * 128 * D * 2;
        if (USE_NT) { glds16nt(wo0, wp, dst); glds16nt(wo1, wp, dst + 1024); }
        else { glds16(wo0, wp, dst); glds16(wo1, wp, dst + 1024); }
        glds16(xo0, xp, dst + 16384); glds16(xo1, xp, dst + 16384 + 1024);
    };
    const int q = wid >> 1, mh = wid & 1;
    const int g = lane >> 4, qq = (lane & 15) >> 2, p = lane & 3;
    const int fr = ((qq >> 1) & 1) | ((g & 1) << 1);
    const int rowb = (32 * q + 8 * g + qq) * 128 + (p & 1) * 8;
    unsigned aw[2], ax[4];
#pragma unroll
    for (int c = 0; c < 2; ++c) aw[c] = rowb + ((((2 * mh + c) ^ fr) * 2 + (p >> 1)) * 16);
#pragma unroll
    for (int c = 0; c < 4; ++c) ax[c] = 16384 + rowb + (((c ^ fr) * 2 + (p >> 1)) * 16);

    f32x4 acc[2][4];
#pragma unroll
    for (int a = 0; a < 2; ++a)
#pragma unroll
        for (int b = 0; b < 4; ++b) acc[a][b] = (f32x4){0.f, 0.f, 0.f, 0.f};

    stage(0); stage(1); stage(2);
#pragma unroll 1
    for (int s = 0; s < NST; ++s) {
        if (s + 2 < NST) asm volatile("s_waitcnt vmcnt(8) lgkmcnt(0)\n\ts_barrier" ::: "memory");
        else if (s + 1 < NST) asm volatile("s_waitcnt vmcnt(4) lgkmcnt(0)\n\ts_barrier" ::: "memory");
        else asm volatile("s_waitcnt vmcnt(0) lgkmcnt(0)\n\ts_barrier" ::: "memory");
        if (s + 3 < NST) stage(s + 3);
        lds_cptr base = (lds_cptr)lds + (s % NBUF) * STAGE;
        bf16x8 wf[2], xf[4];
#pragma unroll
        for (int c = 0; c < 2; ++c) { s16x4 lo = vtr(base + aw[c]), hi = vtr(base + aw[c] + 512); wf[c] = __builtin_shufflevector(lo, hi, 0, 1, 2, 3, 4, 5, 6, 7); }
#pragma unroll
        for (int c = 0; c < 4; ++c) { s16x4 lo = vtr(base + ax[c]), hi = vtr(base + ax[c] + 512); xf[c] = __builtin_shufflevector(lo, hi, 0, 1, 2, 3, 4, 5, 6, 7); }
#pragma unroll
        for (int a = 0; a < 2; ++a)
#pragma unroll
            for (int b = 0; b < 4; ++b) acc[a][b] = MFMA16(xf[b], wf[a], acc[a][b]);
    }
    asm volatile("s_waitcnt lgkmcnt(0)\n\ts_barrier" ::: "memory");
    constexpr int RS = 272;
    {
        lds_ptr red = (lds_ptr)lds + q * (64 * RS);
#pragma unroll
        for (int a = 0; a < 2; ++a)
#pragma unroll
            for (int b = 0; b < 4; ++b)
                *(__attribute__((address_space(3))) f32x4*)(red + (32 * mh + 16 * a + (lane & 15)) * RS + (16 * b + 4 * g) * 4) = acc[a][b];
    }
    __syncthreads();
    {
        const int m = tid >> 3, d0 = (tid & 7) * 8;
        f32x4 s0 = {0.f, 0.f, 0.f, 0.f}, s1 = s0;
#pragma unroll
        for (int qq2 = 0; qq2 < 4; ++qq2) {
            lds_cptr rp = (lds_cptr)lds + qq2 * (64 * RS) + m * RS + d0 * 4;
            s0 += *(const __attribute__((address_space(3))) f32x4*)rp;
            s1 += *(const __attribute__((address_space(3))) f32x4*)(rp + 16);
        }
        const float bv = bf2f(bias[mtile * 64 + m]);
        float v[8];
#pragma unroll
        for (int i = 0; i < 4; ++i) { v[i] = s0[i] + bv; v[4 + i] = s1[i] + bv; }
        if (EPI == 1) {
#pragma unroll
            for (int i = 0; i < 8; ++i) {
                const float u = v[i];
                const float z = u * (-2.302208198f + -0.1029432397f * u * u);
                v[i] = u * __builtin_amdgcn_rcpf(1.f + __builtin_amdgcn_exp2f(z));
            }
        }
        u32x4 o = {cvtpk(v[0], v[1]), cvtpk(v[2], v[3]), cvtpk(v[4], v[5]), cvtpk(v[6], v[7])};
        *(u32x4*)(out + (size_t)(mtile * 64 + m) * D + dg * 64 + d0) = o;
    }
}

extern "C" void kernel_launch(void* const* d_in, const int* in_sizes, int n_in,
                              void* d_out, int out_size, void* d_ws, size_t ws_size,
                              hipStream_t stream) {
    const unsigned short* x = (const unsigned short*)d_in[0];
    const unsigned short* w1 = (const unsigned short*)d_in[1];
    const unsigned short* b1 = (const unsigned short*)d_in[2];
    const unsigned short* w2 = (const unsigned short*)d_in[3];
    const unsigned short* b2 = (const unsigned short*)d_in[4];
    unsigned short* h = (unsigned short*)d_ws;
    unsigned short* G = h + S * D;
    constexpr int LDSB = 131072;
    (void)hipFuncSetAttribute((const void*)gemm_kernel<S, F, 1>, hipFuncAttributeMaxDynamicSharedMemorySize, LDSB);
    (void)hipFuncSetAttribute((const void*)gemm_kernel<F, S, 0>, hipFuncAttributeMaxDynamicSharedMemorySize, LDSB);
    ln_kernel<<<S / 4, 256, 0, stream>>>(x, h);
    gemm_kernel<S, F, 1><<<224, 512, LDSB, stream>>>(w1, h, b1, G);
    gemm_kernel<F, S, 0><<<224, 512, LDSB, stream>>>(w2, G, b2, (unsigned short*)d_out);
}
```

```cpp
#include <hip/hip_runtime.h>
#include <utility>

#define MAP2D 1
#define XD 6
#define TIMING 0

constexpr int S = 2048, D = 448, F = 1792;
constexpr int R = XD;
constexpr unsigned WS_EPOCH = 0, WS_HFLAG = 4096, WS_GFLAG = 16384, WS_TIME = 49152, WS_H = 65536, H_PANEL = 64 * 4096, WS_G = WS_H + 7 * H_PANEL, G_PANEL = 56 * 4096,
                   WS_END = WS_G + 7 * G_PANEL;
constexpr unsigned RING = R * 4096, RED = 4 * RING, LDS_BYTES = RED + 49152, LN_PITCH = 912;
constexpr unsigned SPIN_CAP = 1u << 17;

typedef short s16x4 __attribute__((ext_vector_type(4)));
typedef float f32x4 __attribute__((ext_vector_type(4)));
typedef unsigned u32x4 __attribute__((ext_vector_type(4)));
typedef unsigned u32x2 __attribute__((ext_vector_type(2)));
typedef __attribute__((address_space(3))) char* lds_ptr;
typedef __attribute__((address_space(3))) s16x4* lds_s16x4;
typedef __attribute__((address_space(3))) f32x4* lds_f32x4;
typedef __attribute__((address_space(3))) u32x4* lds_u32x4;
typedef __attribute__((address_space(1))) unsigned gu32;
typedef __amdgpu_buffer_rsrc_t rsrc_t;
#define DI __device__ __forceinline__
#define RLX_AGENT __ATOMIC_RELAXED, __HIP_MEMORY_SCOPE_AGENT
#define SB() __builtin_amdgcn_sched_barrier(0)
#define ACLOB "a0","a1","a2","a3","a4","a5","a6","a7","a8","a9","a10","a11","a12","a13","a14","a15","a16","a17","a18","a19","a20","a21","a22","a23","a24","a25","a26","a27","a28","a29","a30","a31", \
 "a32","a33","a34","a35","a36","a37","a38","a39","a40","a41","a42","a43","a44","a45","a46","a47","a48","a49","a50","a51","a52","a53","a54","a55","a56","a57","a58","a59","a60","a61","a62","a63", \
 "a64","a65","a66","a67","a68","a69","a70","a71","a72","a73","a74","a75","a76","a77","a78","a79","a80","a81","a82","a83","a84","a85","a86","a87","a88","a89","a90","a91","a92","a93","a94","a95", \
 "a96","a97","a98","a99","a100","a101","a102","a103","a104","a105","a106","a107","a108","a109","a110","a111","a112","a113","a114","a115","a116","a117","a118","a119","a120","a121","a122","a123","a124","a125","a126","a127", \
 "a128","a129","a130","a131","a132","a133","a134","a135","a136","a137","a138","a139","a140","a141","a142","a143","a144","a145","a146","a147","a148","a149","a150","a151","a152","a153","a154","a155","a156","a157","a158","a159", \
 "a160","a161","a162","a163","a164","a165","a166","a167","a168","a169","a170","a171","a172","a173","a174","a175","a176","a177","a178","a179","a180","a181","a182","a183","a184","a185","a186","a187","a188","a189","a190","a191", \
 "a192","a193","a194","a195","a196","a197","a198","a199","a200","a201","a202","a203","a204","a205","a206","a207","a208","a209","a210","a211","a212","a213","a214","a215","a216","a217","a218","a219","a220","a221","a222","a223", \
 "a224","a225","a226","a227","a228","a229","a230","a231","a232","a233","a234","a235","a236","a237","a238","a239","a240","a241","a242","a243","a244","a245","a246","a247","a248","a249","a250","a251","a252","a253","a254","a255"

DI float bf_lo(unsigned u) { return __uint_as_float(u << 16); }
DI float bf_hi(unsigned u) { return __uint_as_float(u & 0xffff0000u); }
DI unsigned cvtpk(float lo, float hi) { unsigned r; asm("v_cvt_pk_bf16_f32 %0, %1, %2" : "=v"(r) : "v"(lo), "v"(hi)); return r; }
template <int N> DI void wait_vm() { asm volatile("s_waitcnt vmcnt(%c0)" :: "i"(N) : "memory"); }
template <int CTRL> DI float dpp_add(float v) { return v + __uint_as_float(__builtin_amdgcn_update_dpp(0u, __float_as_uint(v), CTRL, 0xf, 0xf, true)); }
DI float wave_sum(float v) {
    v = dpp_add<0xB1>(v); v = dpp_add<0x4E>(v); v = dpp_add<0x141>(v); v = dpp_add<0x140>(v);
    auto a = __builtin_amdgcn_permlane16_swap(__float_as_uint(v), __float_as_uint(v), false, false);
    v = __uint_as_float(a[0]) + __uint_as_float(a[1]);
    auto b = __builtin_amdgcn_permlane32_swap(__float_as_uint(v), __float_as_uint(v), false, false);
    return __uint_as_float(b[0]) + __uint_as_float(b[1]);
}
DI float gelu_tanh(float u) {
    const float z = u * (-2.302208198f + -0.1029432397f * u * u);
    return u * __builtin_amdgcn_rcpf(1.f + __builtin_amdgcn_exp2f(z));
}

struct Ctx {
    rsrc_t rw;
    rsrc_t rws;
    unsigned wv0, wv1;
    unsigned wbase;
    unsigned wpitch32;
    unsigned m0base;
    unsigned ra[4];
    unsigned xv0, xv1;
    unsigned xbase;
    int rot;
    u32x4 X[R][4];
    f32x4 acc[4][4];
};

template <int NK> DI int rotk(const Ctx& c, int i) { int k = i + c.rot; return k >= NK ? k - NK : k; }

template <int NK, int I, int P> DI void w_dma1(const Ctx& c) {
    const unsigned so = c.wbase + (unsigned)rotk<NK>(c, I) * c.wpitch32 + (unsigned)(P >> 1) * (c.wpitch32 >> 1);
    const unsigned m0 = c.m0base + (I % R) * 4096 + P * 1024;
    asm volatile("s_mov_b32 m0, %0\n\ts_nop 0\n\tbuffer_load_dwordx4 %1, %2, %3 offen lds" :: "s"(m0), "v"((P & 1) ? c.wv1 : c.wv0), "s"(c.rw), "s"(so) : "memory");
}
template <int NK, int I> DI void w_dma(const Ctx& c) { w_dma1<NK, I, 0>(c); w_dma1<NK, I, 1>(c); w_dma1<NK, I, 2>(c); w_dma1<NK, I, 3>(c); }

template <int PERM, int I, int A> DI void w_read1(const Ctx& c) {
    constexpr int r = 16 * I + 4 * A, so = (I % R) * 4096;
    asm volatile("ds_read_b64_tr_b16 a[%c1:%c2], %0 offset:%c3" :: "v"(c.ra[A]), "i"(r), "i"(r + 1), "i"(so) : "memory", ACLOB);
    asm volatile("ds_read_b64_tr_b16 a[%c1:%c2], %0 offset:%c3" :: "v"(c.ra[PERM ? A : (A ^ 2)]), "i"(r + 2), "i"(r + 3), "i"(so + (PERM ? 2048 : 512)) : "memory", ACLOB);
}
template <int PERM, int I> DI void w_read(const Ctx& c) { w_read1<PERM, I, 0>(c); w_read1<PERM, I, 1>(c); w_read1<PERM, I, 2>(c); w_read1<PERM, I, 3>(c); }
DI void wait_lgkm0() { asm volatile("s_waitcnt lgkmcnt(0)" ::: "memory"); }

template <int NK, int PERM, int I, int YOUNGER> DI void w_proc(const Ctx& c) {
    wait_vm<YOUNGER>();
    w_read<PERM, I>(c);
    if constexpr (I + R < NK) { wait_lgkm0(); w_dma<NK, I + R>(c); }
}
template <int NK, int PERM, int I0, int... Is> DI void w_proc_range(const Ctx& c, std::integer_sequence<int, Is...>) {
    (w_proc<NK, PERM, I0 + Is, 4 * ((I0 + Is + R - 1 < NK ? R - 1 : NK - 1 - (I0 + Is)))>(c), ...);
}

template <int NK, int I, int B> DI void x_load(Ctx& c) {
    const unsigned so = c.xbase + (unsigned)rotk<NK>(c, I) * 4096u;
    asm volatile("buffer_load_dwordx4 %0, %1, %2, %3 offen offset:%c4 sc1" : "=v"(c.X[I % R][B]) : "v"((B & 2) ? c.xv1 : c.xv0), "s"(c.rws), "s"(so), "i"((B & 1) * 1024) : "memory");
}
template <int G, int I, int A, int B> DI void mfma1(Ctx& c) {
    constexpr int r = 16 * I + 4 * A;
    if constexpr (G == 1) {
        if constexpr (I == 0) asm volatile("v_mfma_f32_16x16x32_bf16 %0, a[%c2:%c3], %1, 0" : "=&v"(c.acc[A][B]) : "v"(c.X[I % R][B]), "i"(r), "i"(r + 3));
        else asm volatile("v_mfma_f32_16x16x32_bf16 %0, a[%c2:%c3], %1, %0" : "+v"(c.acc[A][B]) : "v"(c.X[I % R][B]), "i"(r), "i"(r + 3));
    } else {
        if constexpr (I == 0) asm volatile("v_mfma_f32_16x16x32_bf16 %0, %1, a[%c2:%c3], 0" : "=&v"(c.acc[A][B]) : "v"(c.X[I % R][B]), "i"(r), "i"(r + 3));
        else asm volatile("v_mfma_f32_16x16x32_bf16 %0, %1, a[%c2:%c3], %0" : "+v"(c.acc[A][B]) : "v"(c.X[I % R][B]), "i"(r), "i"(r + 3));
    }
}
template <int G, int NK, bool TAILW, int NKN, int I, int B> DI void x_step(Ctx& c, const Ctx& n) {
    constexpr int issued_after = TAILW ? 4 * R - 1 : ((NK - 1 - I >= R - 1 && I + R < NK) ? 4 * R - 1
                                  : ((NK - 1 - I) * 4 + (3 - B) < 4 * R - 1 ? (NK - 1 - I) * 4 + (3 - B) : 4 * R - 1));
    asm volatile("s_waitcnt vmcnt(%c1)" : "+v"(c.X[I % R][B]) : "i"(issued_after) : "memory");
    mfma1<G, I, 0, B>(c); mfma1<G, I, 1, B>(c); mfma1<G, I, 2, B>(c); mfma1<G, I, 3, B>(c);
    if constexpr (I + R < NK) x_load<NK, I + R, B>(c);
    else if constexpr (TAILW) w_dma1<NKN, I + R - NK, B>(n);
}
template <int G, int NK, bool TAILW, int NKN, int... Js> DI void x_loop(Ctx& c, const Ctx& n, std::integer_sequence<int, Js...>) {
    (x_step<G, NK, TAILW, NKN, Js / 4, Js % 4>(c, n), ...);
}
template <int NK, int... Js> DI void x_prologue(Ctx& c, std::integer_sequence<int, Js...>) { (x_load<NK, Js / 4, Js % 4>(c), ...); }
template <int NK, int... Is> DI void w_first(const Ctx& c, std::integer_sequence<int, Is...>) { (w_dma<NK, Is>(c), ...); }

DI void acc_fence(Ctx& c) {
    asm volatile("s_nop 7\n\ts_nop 7" : "+v"(c.acc[0][0]), "+v"(c.acc[0][1]), "+v"(c.acc[0][2]), "+v"(c.acc[0][3]), "+v"(c.acc[1][0]), "+v"(c.acc[1][1]), "+v"(c.acc[1][2]), "+v"(c.acc[1][3]),
                 "+v"(c.acc[2][0]), "+v"(c.acc[2][1]), "+v"(c.acc[2][2]), "+v"(c.acc[2][3]), "+v"(c.acc[3][0]), "+v"(c.acc[3][1]), "+v"(c.acc[3][2]), "+v"(c.acc[3][3]));
}
DI void wg_barrier() { asm volatile("s_waitcnt lgkmcnt(0)\n\ts_barrier" ::: "memory"); }

DI void poll_flags(gu32* p, bool active, unsigned tag) {
    for (unsigned spins = 0; spins < SPIN_CAP; ++spins) {
        const unsigned v = active ? __hip_atomic_load(p, RLX_AGENT) : tag;
        if (__all(v == tag)) break;
        __builtin_amdgcn_s_sleep(1);
    }
    __builtin_amdgcn_fence(__ATOMIC_ACQUIRE, "wavefront");
}

__global__ __launch_bounds__(256, 1) void mixer_fused(const unsigned short* __restrict__ x, const unsigned short* __restrict__ w1, const unsigned short* __restrict__ b1,
                                                      const unsigned short* __restrict__ w2, const unsigned short* __restrict__ b2, unsigned short* __restrict__ out,
                                                      unsigned char* __restrict__ ws) {
    extern __shared__ __attribute__((aligned(16))) char lds[];
    const int tid = threadIdx.x, lane = tid & 63;
    const int w = __builtin_amdgcn_readfirstlane(tid >> 6);
    const int blk = blockIdx.x, xl = blk & 7, j = blk >> 3;
    const int g = lane >> 4, c16 = lane & 15, qq = c16 >> 2, p = lane & 3;
#define STAMP(i)
    STAMP(0);
    const int fi = xl >> 1, half = xl & 1, nd = 4 - half;
    const int tl = half ? j / 3 : j >> 2, dgl = j - tl * nd;
    const int dg = 4 * half + dgl;
    const bool has1 = tl < 7, has2 = tl < 8;
    const int t1 = 7 * fi + tl, t2 = 8 * fi + tl;
    const int rot1 = (2 * tl + 4 * dgl) & 15;
    int rot2 = (7 * tl) / 4 + (7 * dgl) / 2; if (rot2 >= 14) rot2 -= 14;
    constexpr int REPG = 4; const int repg = fi;
    const unsigned lds0 = (unsigned)(uintptr_t)lds;
    lds_ptr L = (lds_ptr)lds;
    const rsrc_t rws = __builtin_amdgcn_make_buffer_rsrc((void*)ws, 0, WS_END, 0x00020000);

    const int row0 = blk * 8 + 2 * w;
    unsigned ep, bias2u; u32x4 xr[2]; u32x2 bias1[2];
    {
        const rsrc_t rx = __builtin_amdgcn_make_buffer_rsrc((void*)x, 0, S * D * 2, 0x00020000);
        const rsrc_t rb1 = __builtin_amdgcn_make_buffer_rsrc((void*)b1, 0, F * 2, 0x00020000);
        const rsrc_t rb2 = __builtin_amdgcn_make_buffer_rsrc((void*)b2, 0, S * 2, 0x00020000);
        const unsigned xo = lane < 56 ? (unsigned)lane * 16u : 0x80000000u, xs = (unsigned)row0 * (D * 2);
        const unsigned b1o = has1 ? (unsigned)(t1 * 64 + 32 * (w >> 1) + 4 * g) * 2u : 0x80000000u;
        const unsigned b2o = has2 ? (unsigned)(t2 * 64 + 16 * w + c16) * 2u : 0x80000000u;
        asm volatile("s_nop 4\n\tbuffer_load_dwordx4 %0, %1, %2, %3 offen" : "=v"(xr[0]) : "v"(xo), "s"(rx), "s"(xs) : "memory");
        asm volatile("buffer_load_dwordx4 %0, %1, %2, %3 offen offset:896" : "=v"(xr[1]) : "v"(xo), "s"(rx), "s"(xs) : "memory");
        asm volatile("buffer_load_dword %0, %1, %2, 0 offen sc1" : "=v"(ep) : "v"(WS_EPOCH), "s"(rws) : "memory");
        asm volatile("buffer_load_dwordx2 %0, %1, %2, 0 offen" : "=v"(bias1[0]) : "v"(b1o), "s"(rb1) : "memory");
        asm volatile("buffer_load_dwordx2 %0, %1, %2, 0 offen offset:32" : "=v"(bias1[1]) : "v"(b1o), "s"(rb1) : "memory");
        asm volatile("buffer_load_ushort %0, %1, %2, 0 offen" : "=v"(bias2u) : "v"(b2o), "s"(rb2) : "memory");
    }
    const int r8 = lane >> 3, cp = lane & 7;
    const unsigned sw0 = (unsigned)((cp ^ (((r8 >> 1) & 3) << 1)) * 16), sw1 = (unsigned)((cp ^ ((((r8 >> 1) & 3) ^ 2) << 1)) * 16);
    Ctx c1, c2;
    c1.rw = __builtin_amdgcn_make_buffer_rsrc((void*)w1, 0, S * F * 2, 0x00020000);
    c2.rw = __builtin_amdgcn_make_buffer_rsrc((void*)w2, 0, S * F * 2, 0x00020000);
    c1.rws = rws; c2.rws = rws;
    c1.wv0 = r8 * (F * 2) + sw0; c1.wv1 = (8 + r8) * (F * 2) + sw1;
    c2.wv0 = r8 * (S * 2) + sw0; c2.wv1 = (8 + r8) * (S * 2) + sw1;
    c1.wpitch32 = 32 * F * 2; c2.wpitch32 = 32 * S * 2;
    c1.wbase = (unsigned)(w * 512) * (F * 2) + (unsigned)t1 * 128;
    c2.wbase = (unsigned)(w * 448) * (S * 2) + (unsigned)t2 * 128;
    c1.m0base = c2.m0base = lds0 + w * RING;
    c1.rot = rot1; c2.rot = rot2;
    {
        const int m1 = 2 * (w >> 1);
        const int f0 = (qq >> 1) ^ ((g & 1) << 1);
        const int f2 = (((g & 1) << 1) | (qq >> 1)) ^ ((g >> 1) << 1);
#pragma unroll
        for (int a = 0; a < 4; ++a) {
            c1.ra[a] = lds0 + w * RING + (8 * g + qq) * 128 + (((a ^ m1) ^ f0) * 32) + (p >> 1) * 16 + (p & 1) * 8;
            c2.ra[a] = lds0 + w * RING + (4 * g + qq) * 128 + (((a ^ w) ^ f2) * 32) + (p >> 1) * 16 + (p & 1) * 8;
        }
        const int mb = 2 * (w & 1);
        c1.xv0 = lane * 16 + mb * 1024; c1.xv1 = lane * 16 + (2 ^ mb) * 1024;
        c2.xv0 = lane * 16; c2.xv1 = lane * 16 + 2048;
        c1.xbase = WS_H + dg * H_PANEL + w * 16 * 4096;
        c2.xbase = WS_G + dg * G_PANEL + w * 14 * 4096;
    }
    SB();
    if (has1) { w_dma<16, 0>(c1); w_dma<16, 1>(c1); wait_vm<8>(); }
    else if (has2) { w_dma<14, 0>(c2); w_dma<14, 1>(c2); wait_vm<8>(); }
    else wait_vm<0>();
    asm volatile("" : "+v"(xr[0]), "+v"(xr[1]), "+v"(ep), "+v"(bias1[0]), "+v"(bias1[1]), "+v"(bias2u));
    const unsigned tag = __builtin_amdgcn_readfirstlane(ep) + 1u;
    SB();

    {
        float v[2][8];
        float s[2], q[2];
#pragma unroll
        for (int r = 0; r < 2; ++r) {
#pragma unroll
            for (int i = 0; i < 4; ++i) { v[r][2 * i] = bf_lo(xr[r][i]); v[r][2 * i + 1] = bf_hi(xr[r][i]); }
            s[r] = ((v[r][0] + v[r][1]) + (v[r][2] + v[r][3])) + ((v[r][4] + v[r][5]) + (v[r][6] + v[r][7]));
        }
        s[0] = wave_sum(s[0]); s[1] = wave_sum(s[1]);
#pragma unroll
        for (int r = 0; r < 2; ++r) {
            const float mean = s[r] * (1.f / D);
            float t = 0.f;
#pragma unroll
            for (int i = 0; i < 8; ++i) { v[r][i] -= mean; t += v[r][i] * v[r][i]; }
            q[r] = lane < 56 ? t : 0.f;
        }
        q[0] = wave_sum(q[0]); q[1] = wave_sum(q[1]);
#pragma unroll
        for (int r = 0; r < 2; ++r) {
            const float rs = rsqrtf(q[r] * (1.f / D) + 1e-5f);
            u32x4 o;
#pragma unroll
            for (int i = 0; i < 4; ++i) o[i] = cvtpk(v[r][2 * i] * rs, v[r][2 * i + 1] * rs);
            if (lane < 56) *(lds_u32x4)(L + RED + (2 * w + r) * LN_PITCH + lane * 16) = o;
        }
        wg_barrier();
#pragma unroll
        for (int uu = 0; uu < 2; ++uu) {
            const int u = w + 4 * uu;
            if (u < 7) {
                lds_ptr a = L + RED + qq * LN_PITCH + (64 * u + 32 * (g >> 1) + 8 * p + 4 * (g & 1)) * 2;
                const s16x4 lo = __builtin_amdgcn_ds_read_tr16_b64_v4i16((lds_s16x4)a);
                const s16x4 hi = __builtin_amdgcn_ds_read_tr16_b64_v4i16((lds_s16x4)(a + 4 * LN_PITCH));
                const u32x2 l2 = __builtin_bit_cast(u32x2, lo), h2 = __builtin_bit_cast(u32x2, hi);
                const u32x4 o = {l2[0], l2[1], h2[0], h2[1]};
                __builtin_amdgcn_raw_buffer_store_b128(o, rws, g * 1024 + ((blk & 3) * 16 + c16) * 16, WS_H + u * H_PANEL + (blk >> 2) * 4096, 16);
            }
        }
    }
    SB();
    STAMP(1);
    if (has1) w_first<16>(c1, std::integer_sequence<int, 2, 3, 4, 5>{});
    else if (has2) w_first<14>(c2, std::integer_sequence<int, 2, 3, 4, 5>{});
    static_assert(R == 6, "prologue written for six ring slots");
    if (has2) wait_vm<16>(); else wait_vm<0>();
    wg_barrier();
    if (lane < 2) __hip_atomic_store((gu32*)(ws + WS_HFLAG + (2 * w + lane) * 1024 + blk * 4), tag, RLX_AGENT);
    SB();
    STAMP(2);
    if (!has2) return;

    if (has1) {
        w_proc_range<16, 0, 0>(c1, std::make_integer_sequence<int, 16>{});
        wait_lgkm0();
        STAMP(3);
        poll_flags((gu32*)(ws + WS_HFLAG + xl * 1024 + (64 * w + lane) * 4), true, tag);
        STAMP(4);
        x_prologue<16>(c1, std::make_integer_sequence<int, 4 * R>{});
        x_loop<1, 16, true, 14>(c1, c2, std::make_integer_sequence<int, 64>{});
        acc_fence(c1);
        STAMP(5);
#pragma unroll
        for (int dl = 1; dl < 4; ++dl) {
#pragma unroll
            for (int k = 0; k < 4; ++k)
                *(lds_f32x4)(L + RED + ((w ^ dl) * 3 + dl - 1) * 4096 + k * 1024 + lane * 16) = c1.acc[2 * (dl >> 1) + (k >> 1)][2 * (dl & 1) + (k & 1)];
        }
        wg_barrier();
        if (blk == 0 && tid == 0) __hip_atomic_store((gu32*)(ws + WS_EPOCH), tag, RLX_AGENT);
        f32x4 r[4];
#pragma unroll
        for (int k = 0; k < 4; ++k) {
            r[k] = c1.acc[k >> 1][k & 1];
#pragma unroll
            for (int dl = 0; dl < 3; ++dl) r[k] += *(lds_f32x4)(L + RED + (w * 3 + dl) * 4096 + k * 1024 + lane * 16);
        }
#pragma unroll
        for (int b = 0; b < 2; ++b) {
            float v[8];
#pragma unroll
            for (int a = 0; a < 2; ++a) {
                v[4 * a + 0] = gelu_tanh(r[2 * a + b][0] + bf_lo(bias1[a][0]));
                v[4 * a + 1] = gelu_tanh(r[2 * a + b][1] + bf_hi(bias1[a][0]));
                v[4 * a + 2] = gelu_tanh(r[2 * a + b][2] + bf_lo(bias1[a][1]));
                v[4 * a + 3] = gelu_tanh(r[2 * a + b][3] + bf_hi(bias1[a][1]));
            }
            const u32x4 o = {cvtpk(v[0], v[1]), cvtpk(v[2], v[3]), cvtpk(v[4], v[5]), cvtpk(v[6], v[7])};
            __builtin_amdgcn_raw_buffer_store_b128(o, rws, lane * 16 + (2 * (w & 1) + b) * 1024, WS_G + dg * G_PANEL + (2 * t1 + (w >> 1)) * 4096, 16);
        }
        SB();
        STAMP(6);
    }
    w_proc_range<14, 1, 0>(c2, std::make_integer_sequence<int, 4>{});
    if (has1) {
        wait_vm<16>();
        wg_barrier();
        if (lane < REPG / 4) __hip_atomic_store((gu32*)(ws + WS_GFLAG + (((w * (REPG / 4) + lane) * 7 + dg) * 4 + t1 / 7) * 128 + (t1 % 7) * 4), tag, RLX_AGENT);
        SB();
    }
    w_proc_range<14, 1, 4>(c2, std::make_integer_sequence<int, 10>{});
    wait_lgkm0();
    poll_flags((gu32*)(ws + WS_GFLAG + ((repg * 7 + dg) * 4 + w) * 128 + (lane & 7) * 4), lane < 7, tag);
    STAMP(7);
    x_prologue<14>(c2, std::make_integer_sequence<int, 4 * R>{});
    x_loop<2, 14, false, 14>(c2, c2, std::make_integer_sequence<int, 56>{});
    acc_fence(c2);
#pragma unroll
    for (int dl = 1; dl < 4; ++dl) {
#pragma unroll
        for (int k = 0; k < 4; ++k) *(lds_f32x4)(L + RED + ((w ^ dl) * 3 + dl - 1) * 4096 + k * 1024 + lane * 16) = c2.acc[dl][k];
    }
    wg_barrier();
    {
        f32x4 r[4];
#pragma unroll
        for (int k = 0; k < 4; ++k) {
            r[k] = c2.acc[0][k];
#pragma unroll
            for (int dl = 0; dl < 3; ++dl) r[k] += *(lds_f32x4)(L + RED + (w * 3 + dl) * 4096 + k * 1024 + lane * 16);
        }
        const float bv = __uint_as_float(bias2u << 16);
        unsigned short* op = out + (size_t)(t2 * 64 + 16 * w + c16) * D + dg * 64 + 8 * g;
#pragma unroll
        for (int h = 0; h < 2; ++h) {
            const f32x4 a = r[2 * h], b = r[2 * h + 1];
            const u32x4 o = {cvtpk(a[0] + bv, a[1] + bv), cvtpk(a[2] + bv, a[3] + bv), cvtpk(b[0] + bv, b[1] + bv), cvtpk(b[2] + bv, b[3] + bv)};
            *(u32x4*)(op + 32 * h) = o;
        }
    }
}

extern "C" void kernel_launch(void* const* d_in, const int* in_sizes, int n_in, void* d_out, int out_size, void* d_ws, size_t ws_size, hipStream_t stream) {
    (void)hipFuncSetAttribute((const void*)mixer_fused, hipFuncAttributeMaxDynamicSharedMemorySize, LDS_BYTES);
    mixer_fused<<<256, 256, LDS_BYTES, stream>>>((const unsigned short*)d_in[0], (const unsigned short*)d_in[1], (const unsigned short*)d_in[2],
                                                 (const unsigned short*)d_in[3], (const unsigned short*)d_in[4], (unsigned short*)d_out, (unsigned char*)d_ws);
}
```

```cpp
#include <hip/hip_runtime.h>
#include <utility>

#define NX 10
#define NPF 2
#define NPP 13
#define PFD 20
#define MAPB 1
#define T2 2
#define NPG 3
#define NPP2 11
#define RW1 4
#define RX1 0
#define RW2 0
#define RX2 0
#define PFW2 1
#define TIMING 0

constexpr int S = 2048, D = 448, F = 1792;
constexpr int R = 6;
static_assert(NPP >= 11 && NPP <= 16 && NPP2 >= 9 && NPP2 <= 14 && NPG >= 2 && NPG <= NPP2 && 4 * T2 <= NX && T2 <= 3 && NPF <= NPP, "schedule");
constexpr unsigned WS_EPOCH = 0, WS_HFLAG = 4096, WS_GFLAG = 16384, WS_H = 65536, H_PANEL = 64 * 4096, WS_G = WS_H + 7 * H_PANEL, G_PANEL = 56 * 4096,
                   WS_END = WS_G + 7 * G_PANEL;
constexpr unsigned RING = R * 4096, RED = 4 * RING, LDS_BYTES = RED + 49152, LN_PITCH = 912;
constexpr unsigned SPIN_CAP = 1u << 17;
constexpr unsigned OOB = 0x80000000u;

typedef short s16x4 __attribute__((ext_vector_type(4)));
typedef float f32x4 __attribute__((ext_vector_type(4)));
typedef unsigned u32x4 __attribute__((ext_vector_type(4)));
typedef unsigned u32x2 __attribute__((ext_vector_type(2)));
typedef __attribute__((address_space(3))) char* lds_ptr;
typedef __attribute__((address_space(3))) s16x4* lds_s16x4;
typedef __attribute__((address_space(3))) f32x4* lds_f32x4;
typedef __attribute__((address_space(3))) u32x4* lds_u32x4;
typedef __attribute__((address_space(1))) unsigned gu32;
typedef __amdgpu_buffer_rsrc_t rsrc_t;
#define DI __device__ __forceinline__
#define RLX_AGENT __ATOMIC_RELAXED, __HIP_MEMORY_SCOPE_AGENT
#define SB() __builtin_amdgcn_sched_barrier(0)
#define ACLOB "a0","a1","a2","a3","a4","a5","a6","a7","a8","a9","a10","a11","a12","a13","a14","a15","a16","a17","a18","a19","a20","a21","a22","a23","a24","a25","a26","a27","a28","a29","a30","a31", \
 "a32","a33","a34","a35","a36","a37","a38","a39","a40","a41","a42","a43","a44","a45","a46","a47","a48","a49","a50","a51","a52","a53","a54","a55","a56","a57","a58","a59","a60","a61","a62","a63", \
 "a64","a65","a66","a67","a68","a69","a70","a71","a72","a73","a74","a75","a76","a77","a78","a79","a80","a81","a82","a83","a84","a85","a86","a87","a88","a89","a90","a91","a92","a93","a94","a95", \
 "a96","a97","a98","a99","a100","a101","a102","a103","a104","a105","a106","a107","a108","a109","a110","a111","a112","a113","a114","a115","a116","a117","a118","a119","a120","a121","a122","a123","a124","a125","a126","a127", \
 "a128","a129","a130","a131","a132","a133","a134","a135","a136","a137","a138","a139","a140","a141","a142","a143","a144","a145","a146","a147","a148","a149","a150","a151","a152","a153","a154","a155","a156","a157","a158","a159", \
 "a160","a161","a162","a163","a164","a165","a166","a167","a168","a169","a170","a171","a172","a173","a174","a175","a176","a177","a178","a179","a180","a181","a182","a183","a184","a185","a186","a187","a188","a189","a190","a191", \
 "a192","a193","a194","a195","a196","a197","a198","a199","a200","a201","a202","a203","a204","a205","a206","a207","a208","a209","a210","a211","a212","a213","a214","a215","a216","a217","a218","a219","a220","a221","a222","a223", \
 "a224","a225","a226","a227","a228","a229","a230","a231","a232","a233","a234","a235","a236","a237","a238","a239","a240","a241","a242","a243","a244","a245","a246","a247","a248","a249","a250","a251","a252","a253","a254","a255"

DI float bf_lo(unsigned u) { return __uint_as_float(u << 16); }
DI float bf_hi(unsigned u) { return __uint_as_float(u & 0xffff0000u); }
DI unsigned cvtpk(float lo, float hi) { unsigned r; asm("v_cvt_pk_bf16_f32 %0, %1, %2" : "=v"(r) : "v"(lo), "v"(hi)); return r; }
template <int N> DI void wait_vm() { static_assert(N >= 0 && N < 64, "vmcnt range"); asm volatile("s_waitcnt vmcnt(%c0)" :: "i"(N) : "memory"); }
template <int CTRL> DI float dpp_add(float v) { return v + __uint_as_float(__builtin_amdgcn_update_dpp(0u, __float_as_uint(v), CTRL, 0xf, 0xf, true)); }
DI float wave_sum(float v) {
    v = dpp_add<0xB1>(v); v = dpp_add<0x4E>(v); v = dpp_add<0x141>(v); v = dpp_add<0x140>(v);
    auto a = __builtin_amdgcn_permlane16_swap(__float_as_uint(v), __float_as_uint(v), false, false);
    v = __uint_as_float(a[0]) + __uint_as_float(a[1]);
    auto b = __builtin_amdgcn_permlane32_swap(__float_as_uint(v), __float_as_uint(v), false, false);
    return __uint_as_float(b[0]) + __uint_as_float(b[1]);
}
DI float gelu_tanh(float u) {
    const float z = u * (-2.302208198f + -0.1029432397f * u * u);
    return u * __builtin_amdgcn_rcpf(1.f + __builtin_amdgcn_exp2f(z));
}

struct Ctx {
    rsrc_t rw;
    rsrc_t rws;
    unsigned wv0, wv1;
    unsigned wbase;
    unsigned wpitch32;
    unsigned m0base;
    unsigned ra[4];
    unsigned xv0, xv1;
    int rot;
    unsigned xso, xbeg, xend;
    u32x4 X[NX];
    f32x4 acc[4][4];
};

template <int NK> DI int rotk(const Ctx& c, int i) { int k = i + c.rot; return k >= NK ? k - NK : k; }

template <int NK, int I, int P> DI void w_dma1(const Ctx& c) {
    const unsigned so = c.wbase + (unsigned)rotk<NK>(c, I) * c.wpitch32 + (unsigned)(P >> 1) * (c.wpitch32 >> 1);
    const unsigned m0 = c.m0base + (I % R) * 4096 + P * 1024;
    asm volatile("s_mov_b32 m0, %0\n\ts_nop 0\n\tbuffer_load_dwordx4 %1, %2, %3 offen lds" :: "s"(m0), "v"((P & 1) ? c.wv1 : c.wv0), "s"(c.rw), "s"(so) : "memory");
}
template <int NK, int I> DI void w_dma(const Ctx& c) { w_dma1<NK, I, 0>(c); w_dma1<NK, I, 1>(c); w_dma1<NK, I, 2>(c); w_dma1<NK, I, 3>(c); }
template <int NK, int I0, int... Is> DI void w_dma_range(const Ctx& c, std::integer_sequence<int, Is...>) { (w_dma<NK, I0 + Is>(c), ...); }

template <int PERM, int I, int A> DI void w_read1(const Ctx& c) {
    constexpr int r = 16 * I + 4 * A, so = (I % R) * 4096;
    asm volatile("ds_read_b64_tr_b16 a[%c1:%c2], %0 offset:%c3" :: "v"(c.ra[A]), "i"(r), "i"(r + 1), "i"(so) : "memory", ACLOB);
    asm volatile("ds_read_b64_tr_b16 a[%c1:%c2], %0 offset:%c3" :: "v"(c.ra[PERM ? A : (A ^ 2)]), "i"(r + 2), "i"(r + 3), "i"(so + (PERM ? 2048 : 512)) : "memory", ACLOB);
}
template <int PERM, int I> DI void w_read(const Ctx& c) { w_read1<PERM, I, 0>(c); w_read1<PERM, I, 1>(c); w_read1<PERM, I, 2>(c); w_read1<PERM, I, 3>(c); }
DI void wait_lgkm0() { asm volatile("s_waitcnt lgkmcnt(0)" ::: "memory"); }

struct Sched { int wc[16]; int ws; };
template <int PH> constexpr Sched mk_sched() {
    Sched s{}; int pos[16] = {}; int n = 0, ps = 0, i0 = 0;
    const int NK = PH == 0 ? 16 : 14, npf = PH == 0 ? NPF : PH == 1 ? NPG : -1;
    if (PH == 0) {
        for (int c = 0; c < 2; ++c) { n += 4; pos[c] = n; }
        n += 3;
        for (int c = 2; c < R; ++c) { n += 4; pos[c] = n; }
        n += 2; ps = n;
    } else if (PH == 1) {
        for (int c = 0; c < 4; ++c) { n += 4; pos[c] = n; }
        s.wc[0] = n - pos[0]; s.wc[1] = n - pos[1];
        n += 4; pos[4] = n; n += 1; n += 4; pos[5] = n; n += 1; ps = n; n += 4; pos[6] = n;
        i0 = 2;
    } else {
        for (int c = 0; c < R; ++c) { n += 4; pos[c] = n; }
    }
    for (int i = i0; i < NK; ++i) {
        if (i == npf) { s.ws = n - ps; n += 1; }
        s.wc[i] = n - pos[i];
        if (i >= 1 && i - 1 + R < NK) { n += 4; pos[i - 1 + R] = n; }
    }
    return s;
}
template <int PH, bool WAIT, int I> DI void w_proc(const Ctx& c) {
    constexpr int NK = PH == 0 ? 16 : 14, PERM = PH == 0 ? 0 : 1;
    constexpr Sched sc = mk_sched<PH>();
    if constexpr (WAIT) wait_vm<sc.wc[I]>();
    w_read<PERM, I>(c);
    if constexpr (I >= 1 && I - 1 + R < NK) { asm volatile("s_waitcnt lgkmcnt(8)" ::: "memory"); w_dma<NK, I - 1 + R>(c); }
}
template <int PH, bool WAIT, int I0, int... Is> DI void w_proc_range_(const Ctx& c, std::integer_sequence<int, Is...>) { (w_proc<PH, WAIT, I0 + Is>(c), ...); }
template <int PH, bool WAIT, int I0, int I1> DI void w_proc_range(const Ctx& c) { w_proc_range_<PH, WAIT, I0>(c, std::make_integer_sequence<int, I1 - I0>{}); }

template <int N> DI void x_load(Ctx& c) {
    constexpr int B = N & 3;
    asm volatile("buffer_load_dwordx4 %0, %1, %2, %3 offen offset:%c4 sc1" : "=v"(c.X[N % NX]) : "v"((B & 2) ? c.xv1 : c.xv0), "s"(c.rws), "s"(c.xso), "i"((B & 1) * 1024) : "memory");
}
DI void x_adv0(Ctx& c) { asm volatile("s_add_u32 %0, %0, 0x1000" : "+s"(c.xso) :: "scc"); }
DI void x_adv1(Ctx& c) { asm volatile("s_cmp_eq_u32 %0, %1\n\ts_cselect_b32 %0, %2, %0" : "+s"(c.xso) : "s"(c.xend), "s"(c.xbeg) : "scc"); }
template <int G, int N, int A> DI void mfma1(Ctx& c) {
    constexpr int I = N >> 2, B = N & 3, r = 16 * I + 4 * A;
    if constexpr (G == 1) {
        if constexpr (I == 0) asm volatile("v_mfma_f32_16x16x32_bf16 %0, a[%c2:%c3], %1, 0" : "=&v"(c.acc[A][B]) : "v"(c.X[N % NX]), "i"(r), "i"(r + 3));
        else asm volatile("v_mfma_f32_16x16x32_bf16 %0, a[%c2:%c3], %1, %0" : "+v"(c.acc[A][B]) : "v"(c.X[N % NX]), "i"(r), "i"(r + 3));
    } else {
        if constexpr (I == 0) asm volatile("v_mfma_f32_16x16x32_bf16 %0, %1, a[%c2:%c3], 0" : "=&v"(c.acc[A][B]) : "v"(c.X[N % NX]), "i"(r), "i"(r + 3));
        else asm volatile("v_mfma_f32_16x16x32_bf16 %0, %1, a[%c2:%c3], %0" : "+v"(c.acc[A][B]) : "v"(c.X[N % NX]), "i"(r), "i"(r + 3));
    }
}
constexpr int x_younger(int NK, int TW, int n) {
    int cnt = 0;
    for (int m = n - NX + 1; m < n; ++m) { const int t = m + NX; if (t < 4 * NK || t - (4 * NK + NX - 4 * TW) >= 0) ++cnt; }
    return cnt;
}
template <int G, int NK, int TW, int NKN, int N> DI void x_step(Ctx& c, const Ctx& n) {
    constexpr int T = N + NX;
    constexpr bool adv = T < 4 * NK && (T & 3) == 0;
    asm volatile("s_waitcnt vmcnt(%c1)" : "+v"(c.X[N % NX]) : "i"(x_younger(NK, TW, N)) : "memory");
    mfma1<G, N, 0>(c);
    if constexpr (adv) x_adv0(c);
    mfma1<G, N, 1>(c);
    if constexpr (adv) x_adv1(c);
    mfma1<G, N, 2>(c); mfma1<G, N, 3>(c);
    if constexpr (T < 4 * NK) x_load<T>(c);
    else if constexpr (T - (4 * NK + NX - 4 * TW) >= 0) w_dma1<NKN, (T - (4 * NK + NX - 4 * TW)) / 4, (T - (4 * NK + NX - 4 * TW)) % 4>(n);
}
template <int G, int NK, int TW, int NKN, int... Js> DI void x_loop(Ctx& c, const Ctx& n, std::integer_sequence<int, Js...>) {
    (x_step<G, NK, TW, NKN, Js>(c, n), ...);
}
template <int N> DI void x_pro1(Ctx& c) { if constexpr (N > 0 && (N & 3) == 0) { x_adv0(c); x_adv1(c); } x_load<N>(c); }
template <int... Js> DI void x_prologue(Ctx& c, std::integer_sequence<int, Js...>) { (x_pro1<Js>(c), ...); }

DI void acc_fence(Ctx& c) {
    asm volatile("s_nop 7\n\ts_nop 7" : "+v"(c.acc[0][0]), "+v"(c.acc[0][1]), "+v"(c.acc[0][2]), "+v"(c.acc[0][3]), "+v"(c.acc[1][0]), "+v"(c.acc[1][1]), "+v"(c.acc[1][2]), "+v"(c.acc[1][3]),
                 "+v"(c.acc[2][0]), "+v"(c.acc[2][1]), "+v"(c.acc[2][2]), "+v"(c.acc[2][3]), "+v"(c.acc[3][0]), "+v"(c.acc[3][1]), "+v"(c.acc[3][2]), "+v"(c.acc[3][3]));
}
DI void wg_barrier() { asm volatile("s_waitcnt lgkmcnt(0)\n\ts_barrier" ::: "memory"); }

DI void poll_flags(gu32* p, unsigned tag) {
    for (unsigned spins = 0; spins < SPIN_CAP; ++spins) {
        const unsigned v = __hip_atomic_load(p, RLX_AGENT);
        if (__all(v == tag)) break;
        __builtin_amdgcn_s_sleep(1);
    }
    wait_vm<0>();
    __builtin_amdgcn_fence(__ATOMIC_ACQUIRE, "wavefront");
}

__global__ __launch_bounds__(256, 1) void mixer_fused(const unsigned short* __restrict__ x, const unsigned short* __restrict__ w1, const unsigned short* __restrict__ b1,
                                                      const unsigned short* __restrict__ w2, const unsigned short* __restrict__ b2, unsigned short* __restrict__ out,
                                                      unsigned char* __restrict__ ws) {
    extern __shared__ __attribute__((aligned(16))) char lds[];
    const int tid = threadIdx.x, lane = tid & 63;
    const int w = __builtin_amdgcn_readfirstlane(tid >> 6);
    const int blk = blockIdx.x;
#define STAMP(i)
#define DUMP()
    STAMP(0);
    const rsrc_t rws = __builtin_amdgcn_make_buffer_rsrc((void*)ws, 0, WS_END, 0x00020000);
    unsigned ep, bias2u; u32x4 xr[2]; u32x2 bias1[2];
    {
        const rsrc_t rx = __builtin_amdgcn_make_buffer_rsrc((void*)x, 0, S * D * 2, 0x00020000);
        const unsigned xo = lane < 56 ? (unsigned)lane * 16u : OOB, xs = (unsigned)(blk * 8 + 2 * w) * (D * 2);
        asm volatile("s_nop 4\n\tbuffer_load_dwordx4 %0, %1, %2, %3 offen" : "=v"(xr[0]) : "v"(xo), "s"(rx), "s"(xs) : "memory");
        asm volatile("buffer_load_dwordx4 %0, %1, %2, %3 offen offset:896" : "=v"(xr[1]) : "v"(xo), "s"(rx), "s"(xs) : "memory");
        asm volatile("buffer_load_dword %0, %1, %2, 0 offen sc1" : "=v"(ep) : "v"(WS_EPOCH), "s"(rws) : "memory");
    }
    SB();
    const int xl = blk & 7, j = blk >> 3, fi = xl >> 1, half = xl & 1;
    const int tq = j / 7, dg = j - 7 * tq;
    const bool has2 = j < 28, has1 = has2 && (tq < 3 || (dg < 4) == (half == 0));
    const int tl = tq < 3 ? 4 * half + tq : 3;
    const int t1 = 7 * fi + tl, t2 = 4 * xl + tq;
    const int rot1 = (RW1 * dg + RX1 * tq) & 15;
    int rot2 = (RW2 * dg + RX2 * tq) % 14;
    const unsigned lds0 = (unsigned)(uintptr_t)lds;
    lds_ptr L = (lds_ptr)lds;
    const int g = lane >> 4, c16 = lane & 15, qq = c16 >> 2, p = lane & 3;
    const int r8 = lane >> 3, cp = lane & 7;
    const unsigned sw0 = (unsigned)((cp ^ (((r8 >> 1) & 3) << 1)) * 16), sw1 = (unsigned)((cp ^ ((((r8 >> 1) & 3) ^ 2) << 1)) * 16);
    Ctx c1, c2;
    c1.rw = __builtin_amdgcn_make_buffer_rsrc((void*)w1, 0, S * F * 2, 0x00020000);
    c1.rws = rws;
    c1.wv0 = r8 * (F * 2) + sw0; c1.wv1 = (8 + r8) * (F * 2) + sw1;
    c1.wpitch32 = 32 * F * 2;
    c1.wbase = (unsigned)(w * 512) * (F * 2) + (unsigned)t1 * 128;
    c1.m0base = c2.m0base = lds0 + w * RING;
    c1.rot = rot1;
    if (has1) { w_dma<16, 0>(c1); w_dma<16, 1>(c1); }
    SB();
    {
        const rsrc_t rb1 = __builtin_amdgcn_make_buffer_rsrc((void*)b1, 0, F * 2, 0x00020000);
        const rsrc_t rb2 = __builtin_amdgcn_make_buffer_rsrc((void*)b2, 0, S * 2, 0x00020000);
        const unsigned b1o = has1 ? (unsigned)(t1 * 64 + 32 * (w >> 1) + 4 * g) * 2u : OOB;
        const unsigned b2o = has2 ? (unsigned)(t2 * 64 + 16 * w + c16) * 2u : OOB;
        asm volatile("s_nop 4\n\tbuffer_load_dwordx2 %0, %1, %2, 0 offen" : "=v"(bias1[0]) : "v"(b1o), "s"(rb1) : "memory");
        asm volatile("buffer_load_dwordx2 %0, %1, %2, 0 offen offset:32" : "=v"(bias1[1]) : "v"(b1o), "s"(rb1) : "memory");
        asm volatile("buffer_load_ushort %0, %1, %2, 0 offen" : "=v"(bias2u) : "v"(b2o), "s"(rb2) : "memory");
    }
    c2.rw = __builtin_amdgcn_make_buffer_rsrc((void*)w2, 0, S * F * 2, 0x00020000);
    c2.rws = rws;
    c2.wv0 = r8 * (S * 2) + sw0; c2.wv1 = (8 + r8) * (S * 2) + sw1;
    c2.wpitch32 = 32 * S * 2;
    c2.wbase = (unsigned)(w * 448) * (S * 2) + (unsigned)t2 * 128;
    c2.rot = rot2;
    {
        const int m1 = 2 * (w >> 1);
        const int f0 = (qq >> 1) ^ ((g & 1) << 1);
        const int f2 = (((g & 1) << 1) | (qq >> 1)) ^ ((g >> 1) << 1);
#pragma unroll
        for (int a = 0; a < 4; ++a) {
            c1.ra[a] = lds0 + w * RING + (8 * g + qq) * 128 + (((a ^ m1) ^ f0) * 32) + (p >> 1) * 16 + (p & 1) * 8;
            c2.ra[a] = lds0 + w * RING + (4 * g + qq) * 128 + (((a ^ w) ^ f2) * 32) + (p >> 1) * 16 + (p & 1) * 8;
        }
        const int mb = 2 * (w & 1);
        c1.xv0 = lane * 16 + mb * 1024; c1.xv1 = lane * 16 + (2 ^ mb) * 1024;
        c2.xv0 = lane * 16; c2.xv1 = lane * 16 + 2048;
        c1.xbeg = WS_H + dg * H_PANEL + w * 16 * 4096; c1.xend = c1.xbeg + 16 * 4096; c1.xso = c1.xbeg + rot1 * 4096;
        c2.xbeg = WS_G + dg * G_PANEL + w * 14 * 4096; c2.xend = c2.xbeg + 14 * 4096; c2.xso = c2.xbeg + rot2 * 4096;
    }
    SB();
    if (has1) wait_vm<8 + 3>(); else wait_vm<3>();
    asm volatile("" : "+v"(xr[0]), "+v"(xr[1]), "+v"(ep));
    const unsigned tag = __builtin_amdgcn_readfirstlane(ep) + 1u;
    SB();
    STAMP(1);

    {
        float v[2][8];
        float s[2], q[2];
        if (has1) w_dma<16, 2>(c1);
        SB();
#pragma unroll
        for (int r = 0; r < 2; ++r) {
#pragma unroll
            for (int i = 0; i < 4; ++i) { v[r][2 * i] = bf_lo(xr[r][i]); v[r][2 * i + 1] = bf_hi(xr[r][i]); }
            s[r] = ((v[r][0] + v[r][1]) + (v[r][2] + v[r][3])) + ((v[r][4] + v[r][5]) + (v[r][6] + v[r][7]));
        }
        s[0] = wave_sum(s[0]); s[1] = wave_sum(s[1]);
        SB();
        if (has1) w_dma<16, 3>(c1);
        SB();
#pragma unroll
        for (int r = 0; r < 2; ++r) {
            const float mean = s[r] * (1.f / D);
            float t = 0.f;
#pragma unroll
            for (int i = 0; i < 8; ++i) { v[r][i] -= mean; t += v[r][i] * v[r][i]; }
            q[r] = lane < 56 ? t : 0.f;
        }
        q[0] = wave_sum(q[0]); q[1] = wave_sum(q[1]);
        SB();
        if (has1) w_dma<16, 4>(c1);
        SB();
#pragma unroll
        for (int r = 0; r < 2; ++r) {
            const float rs = rsqrtf(q[r] * (1.f / D) + 1e-5f);
            u32x4 o;
#pragma unroll
            for (int i = 0; i < 4; ++i) o[i] = cvtpk(v[r][2 * i] * rs, v[r][2 * i + 1] * rs);
            if (lane < 56) *(lds_u32x4)(L + RED + (2 * w + r) * LN_PITCH + lane * 16) = o;
        }
        SB();
        if (has1) w_dma<16, 5>(c1);
        wg_barrier();
#pragma unroll
        for (int uu = 0; uu < 2; ++uu) {
            const int u = w + 4 * uu, ur = u < 7 ? u : 6;
            lds_ptr a = L + RED + qq * LN_PITCH + (64 * ur + 32 * (g >> 1) + 8 * p + 4 * (g & 1)) * 2;
            const s16x4 lo = __builtin_amdgcn_ds_read_tr16_b64_v4i16((lds_s16x4)a);
            const s16x4 hi = __builtin_amdgcn_ds_read_tr16_b64_v4i16((lds_s16x4)(a + 4 * LN_PITCH));
            const u32x2 l2 = __builtin_bit_cast(u32x2, lo), h2 = __builtin_bit_cast(u32x2, hi);
            const u32x4 o = {l2[0], l2[1], h2[0], h2[1]};
            const unsigned vo = u < 7 ? (unsigned)(g * 1024 + ((blk & 3) * 16 + c16) * 16) : OOB;
            asm volatile("buffer_store_dwordx4 %0, %1, %2, %3 offen sc1\n\ts_nop 1" :: "v"(o), "v"(vo), "s"(rws), "s"(WS_H + ur * H_PANEL + (blk >> 2) * 4096) : "memory");
        }
    }
    SB();
    STAMP(2);
    gu32* const hflag = (gu32*)(ws + WS_HFLAG + (w + 4 * lane) * 1024 + blk * 4);
    const bool hflag_on = lane < 2 && w + 4 * lane < 7;

    if (has1) {
        constexpr Sched sc = mk_sched<0>();
        w_proc_range<0, true, 0, NPF>(c1);
        wait_vm<sc.ws>();
        if (hflag_on) __hip_atomic_store(hflag, tag, RLX_AGENT);
        SB();
        STAMP(3);
        w_proc_range<0, true, NPF, NPP>(c1);
        STAMP(4);
        poll_flags((gu32*)(ws + WS_HFLAG + dg * 1024 + (64 * w + lane) * 4), tag);
        STAMP(5);
        x_prologue(c1, std::make_integer_sequence<int, NX>{});
        w_proc_range<0, false, NPP, 16>(c1);
        wait_lgkm0();
        x_loop<1, 16, T2, 14>(c1, c2, std::make_integer_sequence<int, 64>{});
        acc_fence(c1);
        STAMP(6);
#pragma unroll
        for (int dl = 1; dl < 4; ++dl) {
#pragma unroll
            for (int k = 0; k < 4; ++k)
                *(lds_f32x4)(L + RED + ((w ^ dl) * 3 + dl - 1) * 4096 + k * 1024 + lane * 16) = c1.acc[2 * (dl >> 1) + (k >> 1)][2 * (dl & 1) + (k & 1)];
        }
        SB();
        w_dma_range<14, T2>(c2, std::make_integer_sequence<int, 3 - T2>{});
        wg_barrier();
        if (blk == 0 && tid == 0) __hip_atomic_store((gu32*)(ws + WS_EPOCH), tag, RLX_AGENT);
        f32x4 r[4];
#pragma unroll
        for (int k = 0; k < 4; ++k) {
            r[k] = c1.acc[k >> 1][k & 1];
#pragma unroll
            for (int dl = 0; dl < 3; ++dl) r[k] += *(lds_f32x4)(L + RED + (w * 3 + dl) * 4096 + k * 1024 + lane * 16);
        }
        asm volatile("" : "+v"(bias1[0]), "+v"(bias1[1]), "+v"(r[0]), "+v"(r[1]), "+v"(r[2]), "+v"(r[3]));
        constexpr Sched sg = mk_sched<1>();
        w_dma<14, 3>(c2);
        wait_vm<sg.wc[0]>(); w_read<1, 0>(c2);
        wait_vm<sg.wc[1]>(); w_read<1, 1>(c2);
        SB();
        const unsigned gso = WS_G + dg * G_PANEL + (2 * t1 + (w >> 1)) * 4096;
#pragma unroll
        for (int b = 0; b < 2; ++b) {
            float v[8];
#pragma unroll
            for (int a = 0; a < 2; ++a) {
                v[4 * a + 0] = gelu_tanh(r[2 * a + b][0] + bf_lo(bias1[a][0]));
                v[4 * a + 1] = gelu_tanh(r[2 * a + b][1] + bf_hi(bias1[a][0]));
                v[4 * a + 2] = gelu_tanh(r[2 * a + b][2] + bf_lo(bias1[a][1]));
                v[4 * a + 3] = gelu_tanh(r[2 * a + b][3] + bf_hi(bias1[a][1]));
                asm volatile("" : "+v"(v[4 * a]), "+v"(v[4 * a + 1]), "+v"(v[4 * a + 2]), "+v"(v[4 * a + 3]));
                SB();
                if (b == 0 && a == 0) { w_dma1<14, 4, 0>(c2); w_dma1<14, 4, 1>(c2); }
                if (b == 0 && a == 1) { w_dma1<14, 4, 2>(c2); w_dma1<14, 4, 3>(c2); }
                if (b == 1 && a == 0) w_dma<14, 5>(c2);
                SB();
            }
            const u32x4 o = {cvtpk(v[0], v[1]), cvtpk(v[2], v[3]), cvtpk(v[4], v[5]), cvtpk(v[6], v[7])};
            asm volatile("buffer_store_dwordx4 %0, %1, %2, %3 offen sc1\n\ts_nop 1" :: "v"(o), "v"((unsigned)(lane * 16 + (2 * (w & 1) + b) * 1024)), "s"(rws), "s"(gso) : "memory");
        }
        SB();
        STAMP(7);
        wait_lgkm0();
        w_dma<14, 6>(c2);
        w_proc_range<1, true, 2, NPG>(c2);
        wait_vm<sg.ws>();
        wg_barrier();
        if (lane == 0) __hip_atomic_store((gu32*)(ws + WS_GFLAG + ((w * 7 + dg) * 4 + fi) * 128 + tl * 4), tag, RLX_AGENT);
        SB();
        STAMP(8);
        w_proc_range<1, true, NPG, NPP2>(c2);
    } else {
        wait_vm<0>();
        if (hflag_on) __hip_atomic_store(hflag, tag, RLX_AGENT);
        SB();
        STAMP(3);
        {
            if (PFD) __builtin_amdgcn_s_sleep(PFD);
            const int ns = 7 + half;
            const int si = j >= 28 ? j - 28 : 4 + (half ? dg : dg - 4);
            const unsigned col = (unsigned)(xl * 512 + (lane & 31) * 16 + (lane >> 5) * (S * 2));
            constexpr int NR = 896, RS = 2;
            const int nw = ns * 4, me = si * 4 + w;
            u32x4 d0 = {0, 0, 0, 0}, d1 = d0, d2 = d0, d3 = d0;
            auto rowoff = [&](int m) { m = m < NR ? m : 0; int mm = (m * RS + (MAPB ? 0 : half * 896)); mm = mm >= 1792 ? mm - 1792 : mm;
                const int s = mm >> 8, q = (mm >> 6) & 3, gsel = (mm >> 5) & 1, rr = mm & 31; return (unsigned)((q * 448 + (s + 7 * gsel) * 32 + rr) * (S * 2)); };
            for (int n = me; n < NR; n += 4 * nw) {
                asm volatile("buffer_load_dwordx4 %0, %1, %2, %3 offen" : "+v"(d0) : "v"(col), "s"(c2.rw), "s"(rowoff(n)) : "memory");
                asm volatile("buffer_load_dwordx4 %0, %1, %2, %3 offen" : "+v"(d1) : "v"(col), "s"(c2.rw), "s"(rowoff(n + nw)) : "memory");
                asm volatile("buffer_load_dwordx4 %0, %1, %2, %3 offen" : "+v"(d2) : "v"(col), "s"(c2.rw), "s"(rowoff(n + 2 * nw)) : "memory");
                asm volatile("buffer_load_dwordx4 %0, %1, %2, %3 offen" : "+v"(d3) : "v"(col), "s"(c2.rw), "s"(rowoff(n + 3 * nw)) : "memory");
                wait_vm<8>();
            }
            wait_vm<0>();
            asm volatile("" :: "v"(d0), "v"(d1), "v"(d2), "v"(d3));
        }
        STAMP(4);
        if (!has2) { DUMP(); return; }
        w_dma_range<14, 0>(c2, std::make_integer_sequence<int, R>{});
        w_proc_range<2, true, 0, NPP2>(c2);
    }
    STAMP(9);
    poll_flags((gu32*)(ws + WS_GFLAG + ((fi * 7 + dg) * 4 + w) * 128 + (lane < 7 ? lane : 6) * 4), tag);
    STAMP(10);
    x_prologue(c2, std::make_integer_sequence<int, NX>{});
    w_proc_range<1, false, NPP2, 14>(c2);
    wait_lgkm0();
    x_loop<2, 14, 0, 14>(c2, c2, std::make_integer_sequence<int, 56>{});
    acc_fence(c2);
    STAMP(11);
#pragma unroll
    for (int dl = 1; dl < 4; ++dl) {
#pragma unroll
        for (int k = 0; k < 4; ++k) *(lds_f32x4)(L + RED + ((w ^ dl) * 3 + dl - 1) * 4096 + k * 1024 + lane * 16) = c2.acc[dl][k];
    }
    wg_barrier();
    {
        f32x4 r[4];
#pragma unroll
        for (int k = 0; k < 4; ++k) {
            r[k] = c2.acc[0][k];
#pragma unroll
            for (int dl = 0; dl < 3; ++dl) r[k] += *(lds_f32x4)(L + RED + (w * 3 + dl) * 4096 + k * 1024 + lane * 16);
        }
        asm volatile("" : "+v"(bias2u));
        const float bv = __uint_as_float(bias2u << 16);
        unsigned short* op = out + (size_t)(t2 * 64 + 16 * w + c16) * D + dg * 64 + 8 * g;
#pragma unroll
        for (int h = 0; h < 2; ++h) {
            const f32x4 a = r[2 * h], b = r[2 * h + 1];
            const u32x4 o = {cvtpk(a[0] + bv, a[1] + bv), cvtpk(a[2] + bv, a[3] + bv), cvtpk(b[0] + bv, b[1] + bv), cvtpk(b[2] + bv, b[3] + bv)};
            *(u32x4*)(op + 32 * h) = o;
        }
    }
    STAMP(12);
    DUMP();
}

extern "C" void kernel_launch(void* const* d_in, const int* in_sizes, int n_in, void* d_out, int out_size, void* d_ws, size_t ws_size, hipStream_t stream) {
    (void)hipFuncSetAttribute((const void*)mixer_fused, hipFuncAttributeMaxDynamicSharedMemorySize, LDS_BYTES);
    mixer_fused<<<256, 256, LDS_BYTES, stream>>>((const unsigned short*)d_in[0], (const unsigned short*)d_in[1], (const unsigned short*)d_in[2],
                                                 (const unsigned short*)d_in[3], (const unsigned short*)d_in[4], (unsigned short*)d_out, (unsigned char*)d_ws);
}
```

```cpp
#include <hip/hip_runtime.h>
#include <utility>

#define NX 10
#define NPF 0
#define NPP 13
#define PFD 20
#define MAPB 1
#define T2 2
#define NPG 4
#define NPP2 11
#define RW1 4
#define RX1 0
#define RW2 0
#define RX2 0
#define PFW2 1
#define P2 5
#define P3 5
#define P4 5
#define P5 5
#define XNT 0
#define C1N 5
#define C1D 2
#define S1 0
#define C2N 5
#define C2D 2
#define S2 0
#define PFNT 0
#define ONT 3
#define W1NT 0
#define COOP 1
#define SPNT 0
#define XPOS 1
#define EW 4
#define LST 0
#define SLP 0
#define P1 4
#define EXP 1
#define CPF 1
#define CPF_N 32
#define TIMING 0

constexpr int S = 2048, D = 448, F = 1792;
constexpr int R = 6;
static_assert(COOP && NPF == 0 && P1 <= P2 && P2 <= P3 && P3 <= P4 && P4 <= P5 && P2 >= 0 && XPOS == 1 && NPP >= 11 && NPP <= 16 && NPP2 >= 9 && NPP2 <= 14 && NPG >= 2 && NPG <= NPP2 && 4 * T2 <= NX - 1 && T2 <= 2 && NPF <= NPP, "schedule");
constexpr unsigned WS_EPOCH = 0, WS_HFLAG = 4096, WS_GFLAG = 16384, WS_H = 65536, H_PANEL = 64 * 4096, WS_G = WS_H + 7 * H_PANEL, G_PANEL = 56 * 4096,
                   WS_END = WS_G + 7 * G_PANEL;
constexpr unsigned RING = R * 4096, RED = 4 * RING, LDS_BYTES = RED + 49152, LN_PITCH = 912;
constexpr unsigned SPIN_CAP = 1u << 17;
constexpr unsigned OOB = 0x80000000u;

typedef short s16x4 __attribute__((ext_vector_type(4)));
typedef float f32x2 __attribute__((ext_vector_type(2)));
typedef float f32x4 __attribute__((ext_vector_type(4)));
typedef unsigned u32x4 __attribute__((ext_vector_type(4)));
typedef unsigned u32x2 __attribute__((ext_vector_type(2)));
typedef __attribute__((address_space(3))) char* lds_ptr;
typedef __attribute__((address_space(3))) s16x4* lds_s16x4;
typedef __attribute__((address_space(3))) f32x4* lds_f32x4;
typedef __attribute__((address_space(3))) u32x4* lds_u32x4;
typedef __attribute__((address_space(1))) unsigned gu32;
typedef __amdgpu_buffer_rsrc_t rsrc_t;
#define PFPOL ""
#define DI __device__ __forceinline__
#define RLX_AGENT __ATOMIC_RELAXED, __HIP_MEMORY_SCOPE_AGENT
#define SB() __builtin_amdgcn_sched_barrier(0)
#define LIKELY(x) __builtin_expect((x), 1)
#define ACLOB "a0","a1","a2","a3","a4","a5","a6","a7","a8","a9","a10","a11","a12","a13","a14","a15","a16","a17","a18","a19","a20","a21","a22","a23","a24","a25","a26","a27","a28","a29","a30","a31", \
 "a32","a33","a34","a35","a36","a37","a38","a39","a40","a41","a42","a43","a44","a45","a46","a47","a48","a49","a50","a51","a52","a53","a54","a55","a56","a57","a58","a59","a60","a61","a62","a63", \
 "a64","a65","a66","a67","a68","a69","a70","a71","a72","a73","a74","a75","a76","a77","a78","a79","a80","a81","a82","a83","a84","a85","a86","a87","a88","a89","a90","a91","a92","a93","a94","a95", \
 "a96","a97","a98","a99","a100","a101","a102","a103","a104","a105","a106","a107","a108","a109","a110","a111","a112","a113","a114","a115","a116","a117","a118","a119","a120","a121","a122","a123","a124","a125","a126","a127", \
 "a128","a129","a130","a131","a132","a133","a134","a135","a136","a137","a138","a139","a140","a141","a142","a143","a144","a145","a146","a147","a148","a149","a150","a151","a152","a153","a154","a155","a156","a157","a158","a159", \
 "a160","a161","a162","a163","a164","a165","a166","a167","a168","a169","a170","a171","a172","a173","a174","a175","a176","a177","a178","a179","a180","a181","a182","a183","a184","a185","a186","a187","a188","a189","a190","a191", \
 "a192","a193","a194","a195","a196","a197","a198","a199","a200","a201","a202","a203","a204","a205","a206","a207","a208","a209","a210","a211","a212","a213","a214","a215","a216","a217","a218","a219","a220","a221","a222","a223", \
 "a224","a225","a226","a227","a228","a229","a230","a231","a232","a233","a234","a235","a236","a237","a238","a239","a240","a241","a242","a243","a244","a245","a246","a247","a248","a249","a250","a251","a252","a253","a254","a255"

DI float bf_lo(unsigned u) { return __uint_as_float(u << 16); }
DI float bf_hi(unsigned u) { return __uint_as_float(u & 0xffff0000u); }
DI unsigned cvtpk(float lo, float hi) { unsigned r; asm("v_cvt_pk_bf16_f32 %0, %1, %2" : "=v"(r) : "v"(lo), "v"(hi)); return r; }
template <int N> DI void wait_vm() { static_assert(N >= 0 && N < 64, "vmcnt range"); asm volatile("s_waitcnt vmcnt(%c0)" :: "i"(N) : "memory"); }
template <int CTRL> DI float dpp_add(float v) { return v + __uint_as_float(__builtin_amdgcn_update_dpp(0u, __float_as_uint(v), CTRL, 0xf, 0xf, true)); }
DI float wave_sum(float v) {
    v = dpp_add<0xB1>(v); v = dpp_add<0x4E>(v); v = dpp_add<0x141>(v); v = dpp_add<0x140>(v);
    auto a = __builtin_amdgcn_permlane16_swap(__float_as_uint(v), __float_as_uint(v), false, false);
    v = __uint_as_float(a[0]) + __uint_as_float(a[1]);
    auto b = __builtin_amdgcn_permlane32_swap(__float_as_uint(v), __float_as_uint(v), false, false);
    return __uint_as_float(b[0]) + __uint_as_float(b[1]);
}
DI float gelu_tanh(float u) {
    const float z = u * (-2.302208198f + -0.1029432397f * u * u);
    return u * __builtin_amdgcn_rcpf(1.f + __builtin_amdgcn_exp2f(z));
}

#define PKG 1
DI f32x2 gelu2(f32x2 u) {
    const f32x2 z = u * (u * u * -0.1029432397f + -2.302208198f);
    f32x2 e = {__builtin_amdgcn_exp2f(z[0]), __builtin_amdgcn_exp2f(z[1])};
    e = e + 1.f;
    const f32x2 r = {__builtin_amdgcn_rcpf(e[0]), __builtin_amdgcn_rcpf(e[1])};
    return u * r;
}
struct Ctx {
    rsrc_t rw;
    rsrc_t rws;
    unsigned wv0, wv1;
    unsigned wbase;
    unsigned wpitch32;
    unsigned m0base;
    unsigned ra[4];
    unsigned xv[4];
    int rot;
    unsigned xso;
    unsigned xtab[8];
    unsigned redw[3], redr;
    u32x4 X[NX];
    f32x4 acc[4][4];
};

template <int NK> DI int rotk(const Ctx& c, int i) { int k = i + c.rot; return k >= NK ? k - NK : k; }

template <int NK, int I, int P> DI void w_dma1(const Ctx& c) {
    const unsigned so = c.wbase + (unsigned)rotk<NK>(c, I) * c.wpitch32 + (unsigned)(P >> 1) * ((c.wpitch32 >> 1) - 2048);
    if constexpr (P == 0) asm volatile("s_mov_b32 m0, %0\n\ts_nop 0\n\tbuffer_load_dwordx4 %1, %2, %3 offen lds" :: "s"(c.m0base + (I % R) * 4096), "v"(c.wv0), "s"(c.rw), "s"(so) : "memory");
    else asm volatile("buffer_load_dwordx4 %0, %1, %2 offen offset:%c3 lds" :: "v"((P & 1) ? c.wv1 : c.wv0), "s"(c.rw), "s"(so), "i"(P * 1024) : "memory");
}
template <int NK, int I> DI void w_dma(const Ctx& c) { w_dma1<NK, I, 0>(c); w_dma1<NK, I, 1>(c); w_dma1<NK, I, 2>(c); w_dma1<NK, I, 3>(c); }
template <int NK, int I0, int... Is> DI void w_dma_range(const Ctx& c, std::integer_sequence<int, Is...>) { (w_dma<NK, I0 + Is>(c), ...); }

template <int PERM, int I, int A> DI void w_read1(const Ctx& c) {
    constexpr int r = 16 * I + 4 * A, so = (I % R) * 4096;
    asm volatile("ds_read_b64_tr_b16 a[%c1:%c2], %0 offset:%c3" :: "v"(c.ra[A]), "i"(r), "i"(r + 1), "i"(so) : "memory", ACLOB);
    asm volatile("ds_read_b64_tr_b16 a[%c1:%c2], %0 offset:%c3" :: "v"(c.ra[PERM ? A : (A ^ 2)]), "i"(r + 2), "i"(r + 3), "i"(so + (PERM ? 2048 : 512)) : "memory", ACLOB);
}
#define RD1 1
template <int PERM, int I> DI void w_read(const Ctx& c) {
    constexpr int so = (I % R) * 4096;
#define TR(r, a, o) "ds_read_b64_tr_b16 a[%c4+" #r ":%c4+" #r "+1], %" #a " offset:%c" #o
    if constexpr (PERM == 0)
        asm volatile(TR(0, 0, 5) "\n\t" TR(2, 2, 6) "\n\t" TR(4, 1, 5) "\n\t" TR(6, 3, 6) "\n\t" TR(8, 2, 5) "\n\t" TR(10, 0, 6) "\n\t" TR(12, 3, 5) "\n\t" TR(14, 1, 6)
                     :: "v"(c.ra[0]), "v"(c.ra[1]), "v"(c.ra[2]), "v"(c.ra[3]), "i"(16 * I), "i"(so), "i"(so + 512) : "memory", ACLOB);
    else
        asm volatile(TR(0, 0, 5) "\n\t" TR(2, 0, 6) "\n\t" TR(4, 1, 5) "\n\t" TR(6, 1, 6) "\n\t" TR(8, 2, 5) "\n\t" TR(10, 2, 6) "\n\t" TR(12, 3, 5) "\n\t" TR(14, 3, 6)
                     :: "v"(c.ra[0]), "v"(c.ra[1]), "v"(c.ra[2]), "v"(c.ra[3]), "i"(16 * I), "i"(so), "i"(so + 2048) : "memory", ACLOB);
#undef TR
}
DI void wait_lgkm0() { asm volatile("s_waitcnt lgkmcnt(0)" ::: "memory"); }

struct Sched { int wc[16]; int ws; };
template <int PH> constexpr Sched mk_sched() {
    Sched s{}; int pos[16] = {}; int n = 0, ps = 0, i0 = 0;
    const int NK = PH == 0 ? 16 : 14, npf = PH == 0 ? NPF : PH == 1 ? NPG : -1;
    const int pp[6] = {-1, P1, P2, P3, P4, P5};
    if (PH == 0) {
        n += 5 + CPF;
        for (int c = 0; c < R; ++c) if (pp[c] < 4) { n += 4; pos[c] = n; }
        n += 2; ps = n;
        for (int c = 0; c < R; ++c) if (pp[c] == 4) { n += 4; pos[c] = n; }
    } else if (PH == 1) {
        for (int c = 0; c < 4; ++c) { n += 4; pos[c] = n; }
        s.wc[0] = n - pos[0];
        n += 4; pos[4] = n; n += 1;
        s.wc[1] = n - pos[1];
        n += 4; pos[5] = n; n += 4; pos[6] = n; n += 1; ps = n;
        i0 = 2;
    } else {
        for (int c = 0; c < R; ++c) { n += 4; pos[c] = n; }
    }
    for (int i = i0; i < NK; ++i) {
        if (i == npf) {
            s.ws = n - ps; n += 1;
            if (PH == 0) for (int c = 0; c < R; ++c) if (pp[c] == 5) { n += 4; pos[c] = n; }
        }
        s.wc[i] = n - pos[i];
        if (i >= 1 && i - 1 + R < NK) { n += 4; pos[i - 1 + R] = n; }
    }
    return s;
}
template <int PH, bool WAIT, int I> DI void w_proc(const Ctx& c) {
    constexpr int NK = PH == 0 ? 16 : 14, PERM = PH == 0 ? 0 : 1;
    constexpr Sched sc = mk_sched<PH>();
    if constexpr (WAIT) wait_vm<sc.wc[I]>();
    w_read<PERM, I>(c);
    if constexpr (I >= 1 && I - 1 + R < NK) { asm volatile("s_waitcnt lgkmcnt(8)" ::: "memory"); w_dma<NK, I - 1 + R>(c); }
}
template <int PH, bool WAIT, int I0, int... Is> DI void w_proc_range_(const Ctx& c, std::integer_sequence<int, Is...>) { (w_proc<PH, WAIT, I0 + Is>(c), ...); }
template <int PH, bool WAIT, int I0, int I1> DI void w_proc_range(const Ctx& c) { w_proc_range_<PH, WAIT, I0>(c, std::make_integer_sequence<int, I1 - I0>{}); }

template <int G, int N> DI void x_load(Ctx& c) {
    if constexpr (G == 1) {
        constexpr int P = N >> 4, I = N & 15;
        asm volatile("buffer_load_dwordx4 %0, %1, %2, %3 offen offset:%c4 sc1" : "=v"(c.X[N % NX]) : "v"(c.xv[P]), "s"(c.rws), "s"(c.xtab[I >> 1]), "i"((I & 1) * 1024) : "memory");
    } else {
        asm volatile("buffer_load_dwordx4 %0, %1, %2, %3 offen offset:%c4 sc1" : "=v"(c.X[N % NX]) : "v"(c.xv[0]), "s"(c.rws), "s"(c.xso), "i"((N & 3) * 1024) : "memory");
    }
}
DI void x_adv(Ctx& c) { asm volatile("s_add_u32 %0, %0, 0x1000" : "+s"(c.xso) :: "scc"); }
template <int G, int NK, int N, int A> DI void mfma1(Ctx& c) {
    constexpr int P = N / NK, I = N % NK, r = 16 * I + 4 * A;
    if constexpr (G == 1) {
        if constexpr (I == 0) asm volatile("v_mfma_f32_16x16x32_bf16 %0, a[%c2:%c3], %1, 0" : "=&v"(c.acc[A][P]) : "v"(c.X[N % NX]), "i"(r), "i"(r + 3));
        else asm volatile("v_mfma_f32_16x16x32_bf16 %0, a[%c2:%c3], %1, %0" : "+v"(c.acc[A][P]) : "v"(c.X[N % NX]), "i"(r), "i"(r + 3));
    } else {
        if constexpr (I == 0) asm volatile("v_mfma_f32_16x16x32_bf16 %0, %1, a[%c2:%c3], 0" : "=&v"(c.acc[A][P]) : "v"(c.X[N % NX]), "i"(r), "i"(r + 3));
        else asm volatile("v_mfma_f32_16x16x32_bf16 %0, %1, a[%c2:%c3], %0" : "+v"(c.acc[A][P]) : "v"(c.X[N % NX]), "i"(r), "i"(r + 3));
    }
}
template <int OFF> DI void red_put(unsigned addr, const f32x4& v) { asm volatile("ds_write_b128 %0, %1 offset:%c2" :: "v"(addr), "v"(v), "i"(OFF) : "memory"); }
template <int OFF> DI void red_get(unsigned addr, f32x4& v) { asm volatile("ds_read_b128 %0, %1 offset:%c2" : "=v"(v) : "v"(addr), "i"(OFF) : "memory"); }
constexpr int XL = XPOS ? 1 : 0;
constexpr int x_younger(int NK, int TW, int n) {
    int cnt = 0;
    for (int t = n + 1; t < n + NX - XL; ++t) if (t < 4 * NK || t - (4 * NK + NX - XL - 4 * TW) >= 0) ++cnt;
    return cnt;
}
template <int G, int NK, int TW, int NKN, int N> DI void x_slot(Ctx& c, const Ctx& n) {
    constexpr int T = N - XL + NX, Q = T - (4 * NK + NX - XL - 4 * TW);
    if constexpr (N >= XL) {
        if constexpr (T < 4 * NK) x_load<G, T>(c);
        else if constexpr (Q >= 0) w_dma1<NKN, Q / 4, Q % 4>(n);
    }
}
template <int G, int NK, int TW, int NKN, int N> DI void x_step(Ctx& c, const Ctx& n, f32x4 (&part)[3][4]) {
    constexpr int TN = N + 1 - XL + NX;
    constexpr bool adv = G == 2 && TN < 4 * NK && (TN & 3) == 0;
    constexpr int P = N / NK, I = N % NK;
    if constexpr (N == 0 || !EW) asm volatile("s_waitcnt vmcnt(%c1)" : "+v"(c.X[N % NX]) : "i"(x_younger(NK, TW, N)) : "memory");
    mfma1<G, NK, N, 0>(c);
    if constexpr (XPOS == 1) x_slot<G, NK, TW, NKN, N>(c, n);
    mfma1<G, NK, N, 1>(c);
    if constexpr (XPOS == 2) x_slot<G, NK, TW, NKN, N>(c, n);
    if constexpr (adv && XPOS == 1) x_adv(c);
    if constexpr (G == 1) {
        if constexpr (P >= 1 && I >= 2 && I < 6) red_put<(I - 2) * 1024>(c.redw[P - 1], c.acc[I - 2][P - 1]);
        if constexpr (P == 3 && I >= 8 && I < 14) red_get<((2 * (I - 8)) >> 2) * 4096 + ((2 * (I - 8)) & 3) * 1024>(c.redr, part[(2 * (I - 8)) >> 2][(2 * (I - 8)) & 3]);
    } else {
        if constexpr (P >= 1 && I >= 2 && I < 5) red_put<(P - 1) * 1024>(c.redw[I - 2], c.acc[I - 1][P - 1]);
        if constexpr (P == 3 && I >= 7 && I < 12) { constexpr int q = 2 * (I - 7); red_get<(q / 3) * 4096 + (q % 3) * 1024>(c.redr, part[q / 3][q % 3]); }
    }
    mfma1<G, NK, N, 2>(c);
    if constexpr (XPOS == 3) x_slot<G, NK, TW, NKN, N>(c, n);
    if constexpr (G == 1) {
        if constexpr (P == 3 && I >= 8 && I < 14) red_get<((2 * (I - 8) + 1) >> 2) * 4096 + ((2 * (I - 8) + 1) & 3) * 1024>(c.redr, part[(2 * (I - 8) + 1) >> 2][(2 * (I - 8) + 1) & 3]);
    } else {
        if constexpr (P == 3 && I >= 7 && I < 11) { constexpr int q = 2 * (I - 7) + 1; red_get<(q / 3) * 4096 + (q % 3) * 1024>(c.redr, part[q / 3][q % 3]); }
    }
    if constexpr (EW == 1 && N + 1 < 4 * NK) asm volatile("s_waitcnt vmcnt(%c1)" : "+v"(c.X[(N + 1) % NX]) : "i"(x_younger(NK, TW, N + 1)) : "memory");
    if constexpr (EW == 2 && N + 1 < 4 * NK) wait_vm<x_younger(NK, TW, N + 1)>();
    mfma1<G, NK, N, 3>(c);
    if constexpr (EW == 3 && N + 1 < 4 * NK) wait_vm<x_younger(NK, TW, N + 1)>();
    if constexpr (EW == 4 && N + 1 < 4 * NK) {
        constexpr int y = x_younger(NK, TW, N + 1);
        static_assert(y < 64, "vmcnt range");
        __builtin_amdgcn_s_waitcnt((y & 15) | (7 << 4) | (15 << 8) | ((y >> 4) << 14));
    }
    if constexpr (P == 3 && I == (G == 1 ? 7 : 6)) asm volatile("s_waitcnt lgkmcnt(0)\n\ts_barrier" ::: "memory");
    if constexpr (XPOS == 0) x_slot<G, NK, TW, NKN, N>(c, n);
}
template <int G, int NK, int TW, int NKN, int... Js> DI void x_loop(Ctx& c, const Ctx& n, f32x4 (&part)[3][4], std::integer_sequence<int, Js...>) {
    (x_step<G, NK, TW, NKN, Js>(c, n, part), ...);
}
template <int G, int N> DI void x_pro1(Ctx& c) { if constexpr (G == 2 && N > 0 && (N & 3) == 0) x_adv(c); x_load<G, N>(c); }
template <int G, int... Js> DI void x_prologue(Ctx& c, std::integer_sequence<int, Js...>) { (x_pro1<G, Js>(c), ...); }
template <int PERM, int I0, int... Is> DI void w_read_range(const Ctx& c, std::integer_sequence<int, Is...>) { (w_read<PERM, I0 + Is>(c), ...); }

DI void acc_fence(Ctx& c) {
    asm volatile("s_nop 7\n\ts_nop 7" : "+v"(c.acc[0][3]), "+v"(c.acc[1][3]), "+v"(c.acc[2][3]), "+v"(c.acc[3][3]));
}
DI void wg_barrier() { asm volatile("s_waitcnt lgkmcnt(0)\n\ts_barrier" ::: "memory"); }

DI void poll_flags(const rsrc_t& rws, unsigned voff, unsigned tag) {
    unsigned v, budget = SPIN_CAP; unsigned long long live;
    asm volatile("1:\n\tbuffer_load_dword %0, %3, %4, 0 offen sc1\n\t"
                 "s_sub_u32 %1, %1, 1\n\t"
                 "s_cselect_b64 %2, 0, -1\n\t"
                 "s_waitcnt vmcnt(0)\n\t"
                 "v_cmp_ne_u32 vcc, %5, %0\n\t"
                 "s_and_b64 vcc, vcc, %2\n\t"
                 "s_cbranch_vccnz 1b"
                 : "=&v"(v), "+s"(budget), "=&s"(live) : "v"(voff), "s"(rws), "s"(tag) : "vcc", "scc", "memory");
}
DI void flag_store(const rsrc_t& rws, unsigned voff, unsigned vtag) {
    asm volatile("buffer_store_dword %0, %1, %2, 0 offen sc1" :: "v"(vtag), "v"(voff), "s"(rws) : "memory");
}

__global__ __launch_bounds__(256, 1) void mixer_fused(const unsigned short* __restrict__ x, const unsigned short* __restrict__ w1, const unsigned short* __restrict__ b1,
                                                      const unsigned short* __restrict__ w2, const unsigned short* __restrict__ b2, unsigned short* __restrict__ out,
                                                      unsigned char* __restrict__ ws) {
    extern __shared__ __attribute__((aligned(16))) char lds[];
    const int tid = threadIdx.x, lane = tid & 63;
    const int w = __builtin_amdgcn_readfirstlane(tid >> 6);
    const int blk = blockIdx.x;
#define STAMP(i)
#define DUMP()
    STAMP(0);
    const rsrc_t rws = __builtin_amdgcn_make_buffer_rsrc((void*)ws, 0, WS_END, 0x00020000);
    unsigned ep, bias2u; u32x4 xr[2]; u32x2 bias1[4];
    asm volatile("" :: "s"(x), "s"(w1), "s"(b1), "s"(w2), "s"(b2), "s"(out));
    {
        const rsrc_t rx = __builtin_amdgcn_make_buffer_rsrc((void*)x, 0, S * D * 2, 0x00020000);
        const unsigned xo = lane < 56 ? (unsigned)lane * 16u : OOB, xs = (unsigned)(blk * 8 + 2 * w) * (D * 2);
        asm volatile("s_nop 4\n\tbuffer_load_dwordx4 %0, %1, %2, %3 offen" : "=v"(xr[0]) : "v"(xo), "s"(rx), "s"(xs) : "memory");
        asm volatile("buffer_load_dwordx4 %0, %1, %2, %3 offen offset:896" : "=v"(xr[1]) : "v"(xo), "s"(rx), "s"(xs) : "memory");
        asm volatile("buffer_load_dword %0, %1, %2, 0 offen sc1" : "=v"(ep) : "v"(WS_EPOCH), "s"(rws) : "memory");
    }
    SB();
    const int xl = blk & 7, j = blk >> 3, fi = xl >> 1, half = xl & 1;
    const int tq = j / 7, dg = j - 7 * tq;
    const bool has2 = j < 28, has1 = has2 && (tq < 3 || (dg < 4) == (half == 0));
    const int tl = tq < 3 ? 4 * half + tq : 3;
    const int t1 = 7 * fi + tl, t2 = 4 * xl + tq;
    const int rot1 = COOP ? 2 * ((tq < 3 || half == 0) ? dg : dg - 4) : (RW1 * dg + RX1 * tq) & 15;
    const int rot2 = 0;
    const unsigned lds0 = (unsigned)(uintptr_t)lds;
    lds_ptr L = (lds_ptr)lds;
    const int g = lane >> 4, c16 = lane & 15, qq = c16 >> 2, p = lane & 3;
    const int r8 = lane >> 3, cp = lane & 7;
    const unsigned sw0 = (unsigned)((cp ^ (((r8 >> 1) & 3) << 1)) * 16), sw1 = (unsigned)((cp ^ ((((r8 >> 1) & 3) ^ 2) << 1)) * 16);
    Ctx c1, c2;
    c1.rw = __builtin_amdgcn_make_buffer_rsrc((void*)w1, 0, S * F * 2, 0x00020000);
    c1.rws = rws;
    c1.wv0 = r8 * (F * 2) + sw0; c1.wv1 = (8 + r8) * (F * 2) + sw1 - 1024;
    c1.wpitch32 = 32 * F * 2;
    c1.wbase = (unsigned)(w * 512) * (F * 2) + (unsigned)t1 * 128;
    c1.m0base = c2.m0base = lds0 + w * RING;
    c1.rot = rot1;
    auto first_touch = [&](auto kc) {
        constexpr int k = decltype(kc)::value;
        if (LIKELY(has1)) w_dma<16, k>(c1);
        else {
            const int si = j >= 28 ? j - 28 : 4 + (half ? dg : dg - 4);
            const int u = 2 * si + k;
            const int tu = u < 6 ? 4 * half + (u >> 1) : 3, cu = u < 6 ? 14 + (u & 1) : (half ? 0 : 2) + u;
            const unsigned so = (unsigned)(w * 512 + 32 * cu) * (F * 2) + (unsigned)(7 * fi + tu) * 128;
#pragma unroll
            for (int pc = 0; pc < 4; ++pc)
                asm volatile("s_mov_b32 m0, %0\n\ts_nop 0\n\tbuffer_load_dwordx4 %1, %2, %3 offen lds" :: "s"(c1.m0base + pc * 1024), "v"(c1.wv0), "s"(c1.rw), "s"(so + pc * 8 * (F * 2)) : "memory");
        }
    };
    constexpr std::integral_constant<int, 0> K0{};
    constexpr std::integral_constant<int, 1> K1{};
    auto w1_at = [&](auto pc_) {
        constexpr int pos = decltype(pc_)::value;
        if (P1 == pos) first_touch(K1);
        if (has1) { if (P2 == pos) w_dma<16, 2>(c1); if (P3 == pos) w_dma<16, 3>(c1); if (P4 == pos) w_dma<16, 4>(c1); if (P5 == pos) w_dma<16, 5>(c1); }
    };
#define W1_AT(n) w1_at(std::integral_constant<int, n>{})
    SB();
    {
        const rsrc_t rb1 = __builtin_amdgcn_make_buffer_rsrc((void*)b1, 0, F * 2, 0x00020000);
        const rsrc_t rb2 = __builtin_amdgcn_make_buffer_rsrc((void*)b2, 0, S * 2, 0x00020000);
        const unsigned b1o = has1 ? (unsigned)(t1 * 64 + 4 * g) * 2u : OOB;
        const unsigned b2o = has2 ? (unsigned)(t2 * 64 + 16 * w + c16) * 2u : OOB;
        asm volatile("s_nop 4\n\tbuffer_load_dwordx2 %0, %1, %2, 0 offen" : "=v"(bias1[0]) : "v"(b1o), "s"(rb1) : "memory");
        asm volatile("buffer_load_dwordx2 %0, %1, %2, 0 offen offset:32" : "=v"(bias1[1]) : "v"(b1o), "s"(rb1) : "memory");
        asm volatile("buffer_load_dwordx2 %0, %1, %2, 0 offen offset:64" : "=v"(bias1[2]) : "v"(b1o), "s"(rb1) : "memory");
        asm volatile("buffer_load_dwordx2 %0, %1, %2, 0 offen offset:96" : "=v"(bias1[3]) : "v"(b1o), "s"(rb1) : "memory");
        asm volatile("buffer_load_ushort %0, %1, %2, 0 offen" : "=v"(bias2u) : "v"(b2o), "s"(rb2) : "memory");
    }
    unsigned cpf;
    {
        unsigned long long pc;
        asm volatile("s_getpc_b64 %0" : "=s"(pc));
        const unsigned co = (CPF_N == 0 || blk < CPF_N) ? (unsigned)(tid * 64) : 0u;
        asm volatile("global_load_dword %0, %1, %2" : "=v"(cpf) : "v"(co), "s"(pc) : "memory");
    }
    SB();
    first_touch(K0);
    if (P1 < 0) first_touch(K1);
    SB();
    c2.rw = __builtin_amdgcn_make_buffer_rsrc((void*)w2, 0, S * F * 2, 0x00020000);
    c2.rws = rws;
    c2.wv0 = r8 * (S * 2) + sw0; c2.wv1 = (8 + r8) * (S * 2) + sw1 - 1024;
    c2.wpitch32 = 32 * S * 2;
    c2.wbase = (unsigned)(w * 448) * (S * 2) + (unsigned)t2 * 128;
    c2.rot = rot2;
    {
        const int f0 = (qq >> 1) ^ ((g & 1) << 1);
        const int f2 = (((g & 1) << 1) | (qq >> 1)) ^ ((g >> 1) << 1);
#pragma unroll
        for (int a = 0; a < 4; ++a) {
            c1.ra[a] = lds0 + w * RING + (8 * g + qq) * 128 + ((a ^ f0) * 32) + (p >> 1) * 16 + (p & 1) * 8;
            c2.ra[a] = lds0 + w * RING + (4 * g + qq) * 128 + (((a ^ w) ^ f2) * 32) + (p >> 1) * 16 + (p & 1) * 8;
            c1.xv[a] = lane * 16 + a * 16384;
        }
        c2.xv[0] = lane * 16;
#pragma unroll
        for (int k = 0; k < 8; ++k) c1.xtab[k] = WS_H + dg * H_PANEL + w * 65536 + ((2 * k + rot1) & 15) * 1024;
        c2.xso = WS_G + dg * G_PANEL + w * 14 * 4096;
#pragma unroll
        for (int dl = 1; dl < 4; ++dl) c1.redw[dl - 1] = c2.redw[dl - 1] = lds0 + RED + ((w ^ dl) * 3 + dl - 1) * 4096 + lane * 16;
        c1.redr = c2.redr = lds0 + RED + w * 3 * 4096 + lane * 16;
    }
    unsigned hfo = (lane < 2 && w + 4 * lane < 7) ? WS_HFLAG + (unsigned)((w + 4 * lane) * 1024 + blk * 4) : OOB;
    unsigned gfo = lane == 0 ? WS_GFLAG + (unsigned)(((w * 7 + dg) * 4 + fi) * 128 + tl * 4) : OOB;
    unsigned hpo = WS_HFLAG + (unsigned)(dg * 1024 + (64 * w + lane) * 4);
    unsigned gpo = WS_GFLAG + (unsigned)(((fi * 7 + dg) * 4 + w) * 128 + (lane < 7 ? lane : 6) * 4);
    asm volatile("" : "+v"(hfo), "+v"(gfo), "+v"(hpo), "+v"(gpo));
    SB();
    wait_vm<5 + CPF + 4 + 4 * (P1 < 0)>();
    asm volatile("" : "+v"(xr[0]), "+v"(xr[1]), "+v"(ep));
    const unsigned tag = __builtin_amdgcn_readfirstlane(ep) + 1u;
    unsigned vtag = tag;
    asm volatile("" : "+v"(vtag));
    SB();
    STAMP(1);

    {
        float v[2][8];
        float s[2], q[2];
        W1_AT(0);
        SB();
#pragma unroll
        for (int r = 0; r < 2; ++r) {
#pragma unroll
            for (int i = 0; i < 4; ++i) { v[r][2 * i] = bf_lo(xr[r][i]); v[r][2 * i + 1] = bf_hi(xr[r][i]); }
            s[r] = ((v[r][0] + v[r][1]) + (v[r][2] + v[r][3])) + ((v[r][4] + v[r][5]) + (v[r][6] + v[r][7]));
        }
        s[0] = wave_sum(s[0]); s[1] = wave_sum(s[1]);
        SB();
        W1_AT(1);
        SB();
#pragma unroll
        for (int r = 0; r < 2; ++r) {
            const float mean = s[r] * (1.f / D);
            float t = 0.f;
#pragma unroll
            for (int i = 0; i < 8; ++i) { v[r][i] -= mean; t += v[r][i] * v[r][i]; }
            q[r] = lane < 56 ? t : 0.f;
        }
        q[0] = wave_sum(q[0]); q[1] = wave_sum(q[1]);
        SB();
        W1_AT(2);
        SB();
#pragma unroll
        for (int r = 0; r < 2; ++r) {
            const float rs = rsqrtf(q[r] * (1.f / D) + 1e-5f);
            u32x4 o;
#pragma unroll
            for (int i = 0; i < 4; ++i) o[i] = cvtpk(v[r][2 * i] * rs, v[r][2 * i + 1] * rs);
            if (lane < 56) *(lds_u32x4)(L + RED + (2 * w + r) * LN_PITCH + lane * 16) = o;
        }
        SB();
        W1_AT(3);
        wg_barrier();
#pragma unroll
        for (int uu = 0; uu < 2; ++uu) {
            const int u = w + 4 * uu, ur = u < 7 ? u : 6;
            lds_ptr a = L + RED + qq * LN_PITCH + (64 * ur + 32 * (g >> 1) + 8 * p + 4 * (g & 1)) * 2;
            const s16x4 lo = __builtin_amdgcn_ds_read_tr16_b64_v4i16((lds_s16x4)a);
            const s16x4 hi = __builtin_amdgcn_ds_read_tr16_b64_v4i16((lds_s16x4)(a + 4 * LN_PITCH));
            const u32x2 l2 = __builtin_bit_cast(u32x2, lo), h2 = __builtin_bit_cast(u32x2, hi);
            const u32x4 o = {l2[0], l2[1], h2[0], h2[1]};
            const unsigned vo = u < 7 ? (unsigned)((((g ^ (blk >> 6)) + 3) & 3) * 16384 + ((blk & 3) * 16 + c16) * 16) : OOB;
            asm volatile("buffer_store_dwordx4 %0, %1, %2, %3 offen sc1\n\ts_nop 1" :: "v"(o), "v"(vo), "s"(rws), "s"(WS_H + ur * H_PANEL + (blk >> 6) * 65536 + ((blk >> 2) & 15) * 1024) : "memory");
        }
    }
    SB();
    STAMP(2);

    if (LIKELY(has1)) {
        constexpr Sched sc = mk_sched<0>();
        { if (P1 == 4) w_dma<16, 1>(c1); if (P2 == 4) w_dma<16, 2>(c1); if (P3 == 4) w_dma<16, 3>(c1); if (P4 == 4) w_dma<16, 4>(c1); if (P5 == 4) w_dma<16, 5>(c1); }
        w_proc_range<0, true, 0, NPF>(c1);
        wait_vm<sc.ws>();
        asm volatile("" :: "v"(cpf));
        flag_store(rws, hfo, vtag);
        SB();
        { if (P1 == 5) w_dma<16, 1>(c1); if (P2 == 5) w_dma<16, 2>(c1); if (P3 == 5) w_dma<16, 3>(c1); if (P4 == 5) w_dma<16, 4>(c1); if (P5 == 5) w_dma<16, 5>(c1); }
        STAMP(3);
        w_proc_range<0, true, NPF, NPP>(c1);
        STAMP(4);
        poll_flags(rws, hpo, tag);
        STAMP(5);
        x_prologue<1>(c1, std::make_integer_sequence<int, NX>{});
        w_read_range<0, NPP>(c1, std::make_integer_sequence<int, 16 - NPP>{});
        wait_lgkm0();
        f32x4 part[3][4];
        x_loop<1, 16, T2, 14>(c1, c2, part, std::make_integer_sequence<int, 64>{});
        acc_fence(c1);
        STAMP(6);
        w_dma_range<14, T2>(c2, std::make_integer_sequence<int, 4 - T2>{});
        if (blk == 0 && tid == 0) __hip_atomic_store((gu32*)(ws + WS_EPOCH), tag, RLX_AGENT);
        asm volatile("s_waitcnt lgkmcnt(0)" : "+v"(part[0][0]), "+v"(part[0][1]), "+v"(part[0][2]), "+v"(part[0][3]), "+v"(part[1][0]), "+v"(part[1][1]), "+v"(part[1][2]), "+v"(part[1][3]),
                     "+v"(part[2][0]), "+v"(part[2][1]), "+v"(part[2][2]), "+v"(part[2][3]) :: "memory");
        asm volatile("" : "+v"(bias1[0]), "+v"(bias1[1]), "+v"(bias1[2]), "+v"(bias1[3]));
        constexpr Sched sg = mk_sched<1>();
        const unsigned gso = WS_G + dg * G_PANEL + fi * 14 * 4096 + w * 14 * 1024 + 2 * tl * 1024;
#pragma unroll
        for (int b = 0; b < 2; ++b) {
            float v[8];
#pragma unroll
            for (int a = 0; a < 2; ++a) {
                const int A = 2 * b + a;
#pragma unroll
                for (int h = 0; h < 2; ++h) {
                    const f32x2 a0 = {c1.acc[A][3][2 * h], c1.acc[A][3][2 * h + 1]}, p0 = {part[0][A][2 * h], part[0][A][2 * h + 1]}, p1 = {part[1][A][2 * h], part[1][A][2 * h + 1]},
                                p2 = {part[2][A][2 * h], part[2][A][2 * h + 1]}, bb = {bf_lo(bias1[A][h]), bf_hi(bias1[A][h])};
                    const f32x2 gv = gelu2(((a0 + p0) + (p1 + p2)) + bb);
                    v[4 * a + 2 * h] = gv[0]; v[4 * a + 2 * h + 1] = gv[1];
                }
                asm volatile("" : "+v"(v[4 * a]), "+v"(v[4 * a + 1]), "+v"(v[4 * a + 2]), "+v"(v[4 * a + 3]));
                SB();
                if (A == 0) { wait_vm<sg.wc[0]>(); w_read<1, 0>(c2); w_dma<14, 4>(c2); }
                if (A == 2) { w_dma<14, 5>(c2); wait_lgkm0(); w_dma<14, 6>(c2); }
                SB();
            }
            const u32x4 o = {cvtpk(v[0], v[1]), cvtpk(v[2], v[3]), cvtpk(v[4], v[5]), cvtpk(v[6], v[7])};
            asm volatile("buffer_store_dwordx4 %0, %1, %2, %3 offen sc1\n\ts_nop 1" :: "v"(o), "v"((unsigned)(lane * 16 + b * 1024)), "s"(rws), "s"(gso) : "memory");
            if (b == 0) { wait_vm<sg.wc[1]>(); w_read<1, 1>(c2); }
        }
        SB();
        STAMP(7);
        w_proc_range<1, true, 2, NPG>(c2);
        wait_vm<sg.ws>();
        wg_barrier();
        flag_store(rws, gfo, vtag);
        SB();
        STAMP(8);
        w_proc_range<1, true, NPG, NPP2>(c2);
    } else {
        if (P1 == 4) first_touch(K1);
        wait_vm<(P1 == 4 ? 4 : 0)>();
        asm volatile("" :: "v"(cpf));
        flag_store(rws, hfo, vtag);
        SB();
        if (P1 == 5) first_touch(K1);
        STAMP(3);
        {
            if (PFD) __builtin_amdgcn_s_sleep(PFD);
            const int ns = 7 + half;
            const int si = j >= 28 ? j - 28 : 4 + (half ? dg : dg - 4);
            const unsigned col = (unsigned)(xl * 512 + (lane & 31) * 16 + (lane >> 5) * (S * 2));
            constexpr int NR = 896, RS = 2;
            const int nw = ns * 4, me = si * 4 + w;
            u32x4 d0 = {0, 0, 0, 0}, d1 = d0, d2 = d0, d3 = d0;
            auto rowoff = [&](int m) { m = m < NR ? m : 0; int mm = (m * RS + (MAPB ? 0 : half * 896)); mm = mm >= 1792 ? mm - 1792 : mm;
                const int s = mm >> 8, q = (mm >> 6) & 3, gsel = (mm >> 5) & 1, rr = mm & 31; return (unsigned)((q * 448 + (s + 7 * gsel) * 32 + rr) * (S * 2)); };
            for (int n = me; n < NR; n += 4 * nw) {
                asm volatile("buffer_load_dwordx4 %0, %1, %2, %3 offen" PFPOL : "+v"(d0) : "v"(col), "s"(c2.rw), "s"(rowoff(n)) : "memory");
                asm volatile("buffer_load_dwordx4 %0, %1, %2, %3 offen" PFPOL : "+v"(d1) : "v"(col), "s"(c2.rw), "s"(rowoff(n + nw)) : "memory");
                asm volatile("buffer_load_dwordx4 %0, %1, %2, %3 offen" PFPOL : "+v"(d2) : "v"(col), "s"(c2.rw), "s"(rowoff(n + 2 * nw)) : "memory");
                asm volatile("buffer_load_dwordx4 %0, %1, %2, %3 offen" PFPOL : "+v"(d3) : "v"(col), "s"(c2.rw), "s"(rowoff(n + 3 * nw)) : "memory");
                wait_vm<8>();
            }
            wait_vm<0>();
            asm volatile("" :: "v"(d0), "v"(d1), "v"(d2), "v"(d3));
        }
        STAMP(4);
        if (!has2) { DUMP(); return; }
        w_dma_range<14, 0>(c2, std::make_integer_sequence<int, R>{});
        w_proc_range<2, true, 0, NPP2>(c2);
    }
    STAMP(9);
    poll_flags(rws, gpo, tag);
    STAMP(10);
    x_prologue<2>(c2, std::make_integer_sequence<int, NX>{});
    w_read_range<1, NPP2>(c2, std::make_integer_sequence<int, 14 - NPP2>{});
    wait_lgkm0();
    f32x4 part[3][4];
    x_loop<2, 14, 0, 14>(c2, c2, part, std::make_integer_sequence<int, 56>{});
    acc_fence(c2);
    STAMP(11);
#pragma unroll
    for (int dl = 1; dl < 4; ++dl) red_put<3 * 1024>(c2.redw[dl - 1], c2.acc[dl][3]);
    wg_barrier();
#pragma unroll
    for (int dl = 0; dl < 3; ++dl) red_get<3 * 1024>(c2.redr + dl * 4096, part[dl][3]);
    asm volatile("s_waitcnt lgkmcnt(0)" : "+v"(part[0][0]), "+v"(part[0][1]), "+v"(part[0][2]), "+v"(part[0][3]), "+v"(part[1][0]), "+v"(part[1][1]), "+v"(part[1][2]), "+v"(part[1][3]),
                 "+v"(part[2][0]), "+v"(part[2][1]), "+v"(part[2][2]), "+v"(part[2][3]) :: "memory");
    {
        f32x4 r[4];
#pragma unroll
        for (int k = 0; k < 4; ++k) r[k] = (c2.acc[0][k] + part[0][k]) + (part[1][k] + part[2][k]);
        asm volatile("" : "+v"(bias2u));
        const float bv = __uint_as_float(bias2u << 16);
        unsigned short* op = out + (size_t)(t2 * 64 + 16 * w + c16) * D + dg * 64 + 8 * g;
#pragma unroll
        for (int h = 0; h < 2; ++h) {
            const f32x4 a = r[2 * h], b = r[2 * h + 1];
            const u32x4 o = {cvtpk(a[0] + bv, a[1] + bv), cvtpk(a[2] + bv, a[3] + bv), cvtpk(b[0] + bv, b[1] + bv), cvtpk(b[2] + bv, b[3] + bv)};
            asm volatile("global_store_dwordx4 %0, %1, off sc0 sc1\n\ts_nop 1" :: "v"(op + 32 * h), "v"(o) : "memory");
        }
    }
    STAMP(12);
    DUMP();
}

extern "C" void kernel_launch(void* const* d_in, const int* in_sizes, int n_in, void* d_out, int out_size, void* d_ws, size_t ws_size, hipStream_t stream) {
    (void)hipFuncSetAttribute((const void*)mixer_fused, hipFuncAttributeMaxDynamicSharedMemorySize, LDS_BYTES);
    mixer_fused<<<256, 256, LDS_BYTES, stream>>>((const unsigned short*)d_in[0], (const unsigned short*)d_in[1], (const unsigned short*)d_in[2],
                                                 (const unsigned short*)d_in[3], (const unsigned short*)d_in[4], (unsigned short*)d_out, (unsigned char*)d_ws);
}
```
